```python
import torch

def _softmax(x, axis=-1):
    return torch.softmax(x, dim=axis)
def _gelu(x):
    return torch.nn.functional.gelu(x, approximate="tanh")
def _silu(x):
    return torch.nn.functional.silu(x)
def _sigmoid(x):
    return torch.sigmoid(x)
def _layernorm(x, axis=-1, eps=1e-5):
    m = x.mean(dim=axis, keepdim=True)
    v = x.var(dim=axis, keepdim=True, unbiased=False)
    return (x - m) / torch.sqrt(v + eps)
def _scatter_add(tgt, idx, src, axis=0):
    return tgt.clone().index_add_(axis, idx, src)
def _segment_sum(src, seg, n):
    out = torch.zeros((n,) + src.shape[1:], dtype=src.dtype)
    return out.index_add_(0, seg, src)
def _pad(x, pw):
    flat = tuple(v for pair in reversed(pw) for v in pair)
    return torch.nn.functional.pad(x, flat)

kernel_name = 'gated_mix__spectral_gfnet__swiglu_ffn__prompt'

def setup_inputs():

    _w = (lambda a: (lambda t: t.to(torch.bfloat16) if t.is_floating_point() else t)(torch.as_tensor(a)))
    def _A_setup():
        B, S, D = 1, 2048, 512
        Df = D // 2 + 1
        return {
            "x": _w((lambda *s: torch.randn(*s, dtype=torch.bfloat16) * 0.05)(B, S, D)),
            "w_r": _w((lambda *s: torch.randn(*s, dtype=torch.bfloat16) * 0.8)(S, Df) * 0.02),
            "w_i": _w((lambda *s: torch.randn(*s, dtype=torch.bfloat16) * 0.8)(S, Df) * 0.02),
        }
    _A_in = _A_setup()

    def _B_setup():
        B, S, D, F = 1, 2048, 512, 1376
        return {
            "x": _w((lambda *s: torch.randn(*s, dtype=torch.bfloat16) * 0.05)(B, S, D)),
            "w_gate": _w((lambda *s: torch.randn(*s, dtype=torch.bfloat16) * 0.05)(D, F)),
            "w_up": _w((lambda *s: torch.randn(*s, dtype=torch.bfloat16) * 0.05)(D, F)),
            "w_down": _w((lambda *s: torch.randn(*s, dtype=torch.bfloat16) * 0.05)(F, D)),
        }
    _B_in = _B_setup()

    return {
        "A_x": _A_in["x"],
        "A_w_r": _A_in["w_r"],
        "A_w_i": _A_in["w_i"],
        "B_x": _B_in["x"],
        "B_w_gate": _B_in["w_gate"],
        "B_w_up": _B_in["w_up"],
        "B_w_down": _B_in["w_down"],
        "gate_w": _w((lambda *s: torch.randn(*s, dtype=torch.bfloat16) * 0.05)(512) * 16.0),
    }

def reference(A_x, A_w_r, A_w_i, B_x, B_w_gate, B_w_up, B_w_down, gate_w):
    def _A_ref(x, w_r, w_i):
        B, S, D = x.shape
        X = (lambda x, axes=(-2,-1): torch.fft.rfft2(x.float(), dim=axes))(x, axes=(-2, -1))
        W = w_r + 1j * w_i
        X = X * W
        y = (lambda x, s, axes=(-2,-1): torch.fft.irfft2(x, s=s, dim=axes).to(torch.bfloat16))(X, (S, D), axes=(-2, -1))
        return y

    def _B_ref(x, w_gate, w_up, w_down):
        g = _silu(x @ w_gate)
        u = x @ w_up
        return (g * u) @ w_down

    x = A_x
    g = _sigmoid((x * gate_w).sum(-1, keepdims=True))
    a = _A_ref(x, A_w_r, A_w_i)
    b = _B_ref(x, B_w_gate, B_w_up, B_w_down)
    return g * a + (1 - g) * b
```

```cpp
#include <hip/hip_runtime.h>
#include <hip/hip_bf16.h>
#include <stdint.h>

typedef __attribute__((ext_vector_type(8))) short bf16x8;
typedef __attribute__((ext_vector_type(4))) short bf16x4;
typedef __attribute__((ext_vector_type(4))) float f32x4;
typedef __attribute__((ext_vector_type(2))) float f32x2;
typedef unsigned short u16;
typedef unsigned int u32;
typedef __attribute__((address_space(3))) bf16x4* lds_b4_ptr;

constexpr int SEQ = 2048, DM = 512, DF = 257, FF = 1376;
constexpr int KS32 = FF / 32;

constexpr size_t WS_YT = 0;
constexpr size_t WS_WT = WS_YT + (size_t)DF * SEQ * 8;
constexpr size_t WS_H = WS_WT + (size_t)DF * SEQ * 4;
constexpr size_t WS_WD = WS_H + (size_t)SEQ * FF * 2;
constexpr size_t WS_B = WS_WD + (size_t)FF * DM * 2;
constexpr size_t WS_G = WS_B + (size_t)SEQ * DM * 2;

__device__ __forceinline__ float bf2f(u16 v) { return __uint_as_float(((u32)v) << 16); }
__device__ __forceinline__ u16 f2bf(float f) {
  __bf16 b = (__bf16)f;
  return __builtin_bit_cast(u16, b);
}
__device__ __forceinline__ float bfr(float f) { return bf2f(f2bf(f)); }

__device__ __forceinline__ f32x2 cmul(f32x2 a, f32x2 b) {
  f32x2 r;
  r.x = a.x * b.x - a.y * b.y;
  r.y = a.x * b.y + a.y * b.x;
  return r;
}
__device__ __forceinline__ f32x2 cmulc(f32x2 a, f32x2 b) {
  f32x2 r;
  r.x = a.x * b.x + a.y * b.y;
  r.y = a.y * b.x - a.x * b.y;
  return r;
}
__device__ __forceinline__ f32x2 twid(float frac) {
  f32x2 r;
  r.x = __builtin_amdgcn_cosf(frac);
  r.y = -__builtin_amdgcn_sinf(frac);
  return r;
}

template <int SGN>
__device__ __forceinline__ f32x2 mul_i(f32x2 z) {
  f32x2 r;
  if (SGN < 0) { r.x = z.y; r.y = -z.x; } else { r.x = -z.y; r.y = z.x; }
  return r;
}
template <int SGN>
__device__ __forceinline__ void dft4(f32x2 b0, f32x2 b1, f32x2 b2, f32x2 b3, f32x2& y0, f32x2& y1, f32x2& y2,
                                     f32x2& y3) {
  f32x2 c0 = b0 + b2, c1 = b0 - b2, c2 = b1 + b3, c3 = mul_i<SGN>(b1 - b3);
  y0 = c0 + c2; y2 = c0 - c2; y1 = c1 + c3; y3 = c1 - c3;
}
template <int SGN>
__device__ __forceinline__ void dft8(f32x2 (&v)[8]) {
  const float R = 0.70710678118654752440f;
  f32x2 a0 = v[0] + v[4], a4 = v[0] - v[4];
  f32x2 a1 = v[1] + v[5], a5 = v[1] - v[5];
  f32x2 a2 = v[2] + v[6], a6 = v[2] - v[6];
  f32x2 a3 = v[3] + v[7], a7 = v[3] - v[7];
  f32x2 t5, t7;
  if (SGN < 0) {
    t5.x = (a5.x + a5.y) * R; t5.y = (a5.y - a5.x) * R;
    t7.x = (a7.y - a7.x) * R; t7.y = -(a7.x + a7.y) * R;
  } else {
    t5.x = (a5.x - a5.y) * R; t5.y = (a5.x + a5.y) * R;
    t7.x = -(a7.x + a7.y) * R; t7.y = (a7.x - a7.y) * R;
  }
  f32x2 t6 = mul_i<SGN>(a6);
  dft4<SGN>(a0, a1, a2, a3, v[0], v[2], v[4], v[6]);
  dft4<SGN>(a4, t5, t6, t7, v[1], v[3], v[5], v[7]);
}

__device__ __forceinline__ void wave_sync() {
  __builtin_amdgcn_fence(__ATOMIC_RELEASE, "wavefront");
  __builtin_amdgcn_wave_barrier();
  __builtin_amdgcn_fence(__ATOMIC_ACQUIRE, "wavefront");
}

struct Tw512 {
  f32x2 a[7];
  f32x2 b[7];
};
__device__ __forceinline__ void make_tw512(Tw512& t, int lane) {
#pragma unroll
  for (int k = 1; k < 8; ++k) {
    t.a[k - 1] = twid((float)((lane * k) & 511) * (1.0f / 512.0f));
    t.b[k - 1] = twid((float)(((lane & 7) * k) & 63) * (1.0f / 64.0f));
  }
}

__device__ __forceinline__ void fft512_fwd(f32x2 (&v)[8], f32x2* buf, const Tw512& tw, int lane) {
  const int hi = lane >> 3, lo = lane & 7;
  dft8<-1>(v);
#pragma unroll
  for (int k = 1; k < 8; ++k) v[k] = cmul(v[k], tw.a[k - 1]);
  wave_sync();
#pragma unroll
  for (int k = 0; k < 8; ++k) buf[72 * k + lane] = v[k];
  wave_sync();
#pragma unroll
  for (int n = 0; n < 8; ++n) v[n] = buf[72 * hi + 8 * n + lo];
  dft8<-1>(v);
#pragma unroll
  for (int k = 1; k < 8; ++k) v[k] = cmul(v[k], tw.b[k - 1]);
  wave_sync();
#pragma unroll
  for (int k = 0; k < 8; ++k) buf[72 * hi + 9 * k + lo] = v[k];
  wave_sync();
#pragma unroll
  for (int n = 0; n < 8; ++n) v[n] = buf[72 * hi + 9 * lo + n];
  dft8<-1>(v);
}
__device__ __forceinline__ void fft512_inv(f32x2 (&v)[8], f32x2* buf, const Tw512& tw, int lane) {
  const int hi = lane >> 3, lo = lane & 7;
  dft8<1>(v);
  wave_sync();
#pragma unroll
  for (int n = 0; n < 8; ++n) buf[72 * hi + 9 * lo + n] = v[n];
  wave_sync();
#pragma unroll
  for (int k = 0; k < 8; ++k) v[k] = buf[72 * hi + 9 * k + lo];
#pragma unroll
  for (int k = 1; k < 8; ++k) v[k] = cmulc(v[k], tw.b[k - 1]);
  dft8<1>(v);
  wave_sync();
#pragma unroll
  for (int n = 0; n < 8; ++n) buf[72 * hi + 8 * n + lo] = v[n];
  wave_sync();
#pragma unroll
  for (int k = 0; k < 8; ++k) v[k] = buf[72 * k + lane];
#pragma unroll
  for (int k = 1; k < 8; ++k) v[k] = cmulc(v[k], tw.a[k - 1]);
  dft8<1>(v);
}

__device__ __forceinline__ int sslot(int k) { return k + 4 * (k >> 5); }
constexpr int PS = 584;

constexpr int G1_STAGE = 40960;
constexpr int G1_NST = 3;
constexpr int L1_AUX_OFF = G1_STAGE * G1_NST;
constexpr int L1_LDS = L1_AUX_OFF + 4 * PS * 8;

#define WAIT_V(n) asm volatile("s_waitcnt vmcnt(" #n ")" ::: "memory")
#define WAIT_L(n) asm volatile("s_waitcnt lgkmcnt(" #n ")" ::: "memory")

__global__ __launch_bounds__(512, 2) void k_l1(const u16* __restrict__ x, const u16* __restrict__ wr,
                                               const u16* __restrict__ wi, const u16* __restrict__ wg,
                                               const u16* __restrict__ wu, const u16* __restrict__ wd,
                                               const u16* __restrict__ gw, char* __restrict__ ws) {
  extern __shared__ __attribute__((aligned(16))) char smem[];
  const int tid = threadIdx.x;
  const int lane = tid & 63;
  const int wave = __builtin_amdgcn_readfirstlane(tid >> 6);
  const int bid = blockIdx.x;

  const int xcd = bid & 7, idx = bid >> 3;
  const bool has_g1 = idx < 30;
  const int T = xcd * 30 + idx;
  const int fg = T / 80, rem = T - fg * 80;
  const int mt_ = rem / 5, fi = rem - mt_ * 5;
  const int m0 = mt_ * 128, f0 = (fg * 5 + fi) * 96;

  const u16* xsrc0;
  const u16* xsrc1;
  const u16* wsrc[3];
  {
    const int r0 = (lane >> 3), c = (lane & 7) ^ (lane >> 3);
    xsrc0 = x + (size_t)(m0 + 8 * wave + r0) * DM + c * 8;
    xsrc1 = xsrc0 + (size_t)64 * DM;
    const int rowp = (lane >> 1) & 7, half = lane & 1;
    const int k = wave * 8 + (rowp ^ ((wave & 1) << 2));
#pragma unroll
    for (int i = 0; i < 3; ++i) {
      const int nb = i * 4 + (lane >> 4);
      const int mat = nb >= 6;
      int f = f0 + (nb - mat * 6) * 16 + half * 8;
      f = f > FF - 8 ? FF - 8 : f;
      wsrc[i] = (mat ? wu : wg) + (size_t)k * FF + f;
    }
  }
  auto stage = [&](int kt, int buf) {
    char* base = smem + buf * G1_STAGE;
    __builtin_amdgcn_global_load_lds((const u32*)(xsrc0 + kt * 64), (u32*)(base + wave * 1024), 16, 0, 0);
    __builtin_amdgcn_global_load_lds((const u32*)(xsrc1 + kt * 64), (u32*)(base + (wave + 8) * 1024), 16, 0, 0);
#pragma unroll
    for (int i = 0; i < 3; ++i)
      __builtin_amdgcn_global_load_lds((const u32*)(wsrc[i] + (size_t)kt * 64 * FF),
                                       (u32*)(base + 16384 + (wave * 3 + i) * 1024), 16, 0, 0);
  };

  f32x2* aux = (f32x2*)(smem + L1_AUX_OFF);
  const int s0 = bid * 8;
  if (wave >= 4) {
    const int p = wave - 4;
    const u16* xa = x + (size_t)(s0 + 2 * p) * DM;
    u16 ra[8], rb[8], rg[8];
#pragma unroll
    for (int j = 0; j < 8; ++j) {
      ra[j] = xa[lane + 64 * j];
      rb[j] = xa[DM + lane + 64 * j];
      rg[j] = gw[lane + 64 * j];
    }
    Tw512 tw;
    make_tw512(tw, lane);
    f32x2 v[8];
    float sa = 0.f, sb = 0.f;
#pragma unroll
    for (int j = 0; j < 8; ++j) {
      v[j].x = bf2f(ra[j]);
      v[j].y = bf2f(rb[j]);
      float w = bf2f(rg[j]);
      sa += bfr(v[j].x * w);
      sb += bfr(v[j].y * w);
    }
#pragma unroll
    for (int o = 32; o > 0; o >>= 1) {
      sa += __shfl_xor(sa, o);
      sb += __shfl_xor(sb, o);
    }
    if (lane < 2) {
      float s = bfr(lane ? sb : sa);
      float gg = bfr(1.0f / (1.0f + __expf(-s)));
      ((float*)(ws + WS_G))[s0 + 2 * p + lane] = gg;
    }
    f32x2* buf = aux + p * PS;
    fft512_fwd(v, buf, tw, lane);
    wave_sync();
    const int hi = lane >> 3, lo = lane & 7;
#pragma unroll
    for (int k2 = 0; k2 < 8; ++k2) buf[sslot(hi + 8 * lo + 64 * k2)] = v[k2];
  } else {
    if (has_g1) {
    }
    u32* wt = (u32*)(ws + WS_WT);
    for (int i = tid; i < DF * 2; i += 256) {
      const int kd = i >> 1, h = i & 1;
      uint4 o;
      u32 t[4];
#pragma unroll
      for (int r = 0; r < 4; ++r) {
        const size_t off = (size_t)(s0 + h * 4 + r) * DF + kd;
        t[r] = (u32)wr[off] | ((u32)wi[off] << 16);
      }
      o.x = t[0]; o.y = t[1]; o.z = t[2]; o.w = t[3];
      *(uint4*)(wt + (size_t)kd * SEQ + s0 + h * 4) = o;
    }
    for (int blk = bid * 4 + wave; blk < KS32 * 32; blk += 1024) {
      const int ks = blk >> 5, nt = blk & 31;
      const u16* src = wd + (size_t)(ks * 32 + 8 * (lane >> 4)) * DM + nt * 16 + (lane & 15);
      u16 t[8];
#pragma unroll
      for (int j = 0; j < 8; ++j) t[j] = src[(size_t)j * DM];
      uint4 o;
      o.x = t[0] | ((u32)t[1] << 16); o.y = t[2] | ((u32)t[3] << 16);
      o.z = t[4] | ((u32)t[5] << 16); o.w = t[6] | ((u32)t[7] << 16);
      *(uint4*)(ws + WS_WD + (size_t)blk * 1024 + lane * 16) = o;
    }
  }
  if (has_g1) { stage(0, 0); stage(1, 1); stage(2, 2); }
  WAIT_L(0);
  __builtin_amdgcn_s_barrier();
  {
    f32x2* yt = (f32x2*)(ws + WS_YT);
    for (int i = tid; i < DF * 4; i += 512) {
      const int kd = i >> 2, p = i & 3;
      const f32x2 z1 = aux[p * PS + sslot(kd)];
      const f32x2 z2 = aux[p * PS + sslot((512 - kd) & 511)];
      f32x4 o;
      o.x = 0.5f * (z1.x + z2.x);
      o.y = 0.5f * (z1.y - z2.y);
      o.z = 0.5f * (z1.y + z2.y);
      o.w = 0.5f * (z2.x - z1.x);
      *(f32x4*)(yt + (size_t)kd * SEQ + s0 + 2 * p) = o;
    }
  }
  if (!has_g1) return;

  const int wm = wave >> 1, wf = wave & 1;
  const int l15 = lane & 15, lq = lane >> 4;
  int xoff[2][2];
#pragma unroll
  for (int mt = 0; mt < 2; ++mt)
#pragma unroll
    for (int kk = 0; kk < 2; ++kk) {
      const int row = wm * 32 + mt * 16 + l15;
      const int c = kk * 4 + lq;
      xoff[mt][kk] = row * 128 + ((c ^ (row & 7)) << 4);
    }
  const int wq = l15 >> 2, wp = l15 & 3;
  const int woff0 = 16384 + (lq * 12 + wf * 3) * 256 + ((4 * (lq & 1)) + wq) * 32 + wp * 8;
  const int woff1 = 16384 + (lq * 12 + wf * 3) * 256 + ((4 * ((lq & 1) ^ 1)) + wq) * 32 + wp * 8;

  f32x4 accg[3][2], accu[3][2];
#pragma unroll
  for (int a = 0; a < 3; ++a)
#pragma unroll
    for (int b = 0; b < 2; ++b) { accg[a][b] = (f32x4)0.f; accu[a][b] = (f32x4)0.f; }

  bf16x8 xa[2], wa[6], xb[2], wb[6];
  auto rdfrag = [&](bf16x8(&xf)[2], bf16x8(&wfr)[6], int buf, int kk) {
    const char* base = smem + buf * G1_STAGE;
#pragma unroll
    for (int mt = 0; mt < 2; ++mt) xf[mt] = *(const bf16x8*)(base + xoff[mt][kk]);
#pragma unroll
    for (int n = 0; n < 6; ++n) {
      const int nboff = ((n >= 3 ? 6 : 0) + (n % 3)) * 256 + kk * 4 * 12 * 256;
      bf16x4 lo = __builtin_amdgcn_ds_read_tr16_b64_v4i16((lds_b4_ptr)(base + woff0 + nboff));
      bf16x4 hi = __builtin_amdgcn_ds_read_tr16_b64_v4i16((lds_b4_ptr)(base + woff1 + nboff));
      wfr[n] = __builtin_shufflevector(lo, hi, 0, 1, 2, 3, 4, 5, 6, 7);
    }
  };
  auto mma = [&](bf16x8(&xf)[2], bf16x8(&wfr)[6]) {
#pragma unroll
    for (int ft = 0; ft < 3; ++ft)
#pragma unroll
      for (int mt = 0; mt < 2; ++mt) {
        accg[ft][mt] = __builtin_amdgcn_mfma_f32_16x16x32_bf16(wfr[ft], xf[mt], accg[ft][mt], 0, 0, 0);
        accu[ft][mt] = __builtin_amdgcn_mfma_f32_16x16x32_bf16(wfr[3 + ft], xf[mt], accu[ft][mt], 0, 0, 0);
      }
  };

  WAIT_V(0);
  __builtin_amdgcn_s_barrier();
  rdfrag(xa, wa, 0, 0);
#pragma unroll
  for (int t = 0; t < 8; ++t) {
    rdfrag(xb, wb, t % 3, 1);
    mma(xa, wa);
    if (t < 7) {
      WAIT_L(0);
      if (t + 2 <= 7 && t >= 1) { WAIT_V(5); } else { WAIT_V(0); }
      __builtin_amdgcn_s_barrier();
      if (t + 3 <= 7) stage(t + 3, t % 3);
      rdfrag(xa, wa, (t + 1) % 3, 0);
    }
    mma(xb, wb);
  }

  char* hbase = ws + WS_H;
#pragma unroll
  for (int ft = 0; ft < 3; ++ft) {
    const int f = f0 + (wf * 3 + ft) * 16 + 4 * lq;
    if (f < FF) {
#pragma unroll
      for (int mt = 0; mt < 2; ++mt) {
        const int m = m0 + wm * 32 + mt * 16 + l15;
        u16 o[4];
#pragma unroll
        for (int r = 0; r < 4; ++r) {
          const float g = bfr(accg[ft][mt][r]);
          const float s = bfr(g / (1.0f + __expf(-g)));
          const float u = bfr(accu[ft][mt][r]);
          o[r] = f2bf(s * u);
        }
        uint2 ov;
        ov.x = o[0] | ((u32)o[1] << 16);
        ov.y = o[2] | ((u32)o[3] << 16);
        const size_t off = ((size_t)(m >> 4) * KS32 + (f >> 5)) * 1024 + ((m & 15) + 16 * ((f & 31) >> 3)) * 16 + (f & 7) * 2;
        *(uint2*)(hbase + off) = ov;
      }
    }
  }
}

constexpr int L2_LDS = 49152;

__device__ __forceinline__ void g2_body(char* __restrict__ ws, char* smem, int bid) {
  const int tid = threadIdx.x, lane = tid & 63;
  const int wave = __builtin_amdgcn_readfirstlane(tid >> 6);
  const int xcd = bid & 7, idx = bid >> 3;
  const int mt64 = xcd * 4 + (idx >> 3), nt64 = idx & 7;
  const char* hb = ws + WS_H + (size_t)(mt64 * 4) * KS32 * 1024 + lane * 16;
  const char* wb = ws + WS_WD + (size_t)(nt64 * 4) * 1024 + lane * 16;
  f32x4 acc[4][4];
#pragma unroll
  for (int a = 0; a < 4; ++a)
#pragma unroll
    for (int b = 0; b < 4; ++b) acc[a][b] = (f32x4)0.f;
  bf16x8 hf[3][4], wf[3][4];
  auto ld = [&](int slot, int i) {
    int ks = wave + 4 * i;
    ks = ks > KS32 - 1 ? KS32 - 1 : ks;
#pragma unroll
    for (int j = 0; j < 4; ++j) {
      hf[slot][j] = *(const bf16x8*)(hb + ((size_t)j * KS32 + ks) * 1024);
      wf[slot][j] = *(const bf16x8*)(wb + ((size_t)ks * 32 + j) * 1024);
    }
  };
  ld(0, 0);
  ld(1, 1);
#pragma unroll
  for (int i = 0; i < 11; ++i) {
    if (i + 2 < 11) ld((i + 2) % 3, i + 2);
    if (i < 10 || wave < 3) {
#pragma unroll
      for (int nt = 0; nt < 4; ++nt)
#pragma unroll
        for (int mt = 0; mt < 4; ++mt)
          acc[nt][mt] = __builtin_amdgcn_mfma_f32_16x16x32_bf16(wf[i % 3][nt], hf[i % 3][mt], acc[nt][mt], 0, 0, 0);
    }
  }
  f32x4* red = (f32x4*)smem;
#pragma unroll
  for (int nt = 0; nt < 4; ++nt) {
    if (nt != wave) {
      const int src = wave < nt ? wave : wave - 1;
#pragma unroll
      for (int mt = 0; mt < 4; ++mt) red[((nt * 3 + src) * 4 + mt) * 64 + lane] = acc[nt][mt];
    }
  }
  __syncthreads();
  f32x4 fin[4];
#pragma unroll
  for (int nt = 0; nt < 4; ++nt)
    if (nt == wave) {
#pragma unroll
      for (int mt = 0; mt < 4; ++mt) fin[mt] = acc[nt][mt];
    }
  u16* bo = (u16*)(ws + WS_B);
#pragma unroll
  for (int mt = 0; mt < 4; ++mt) {
#pragma unroll
    for (int s = 0; s < 3; ++s) fin[mt] += red[((wave * 3 + s) * 4 + mt) * 64 + lane];
    const int m = mt64 * 64 + mt * 16 + (lane & 15);
    const int n = nt64 * 64 + wave * 16 + 4 * (lane >> 4);
    uint2 ov;
    ov.x = f2bf(fin[mt][0]) | ((u32)f2bf(fin[mt][1]) << 16);
    ov.y = f2bf(fin[mt][2]) | ((u32)f2bf(fin[mt][3]) << 16);
    *(uint2*)(bo + (size_t)m * DM + n) = ov;
  }
}

__device__ __forceinline__ void k2_body(char* __restrict__ ws, char* smem, int kd) {
  const int tid = threadIdx.x, lane = tid & 63;
  const int wave = __builtin_amdgcn_readfirstlane(tid >> 6);
  f32x2* col = (f32x2*)(ws + WS_YT) + (size_t)kd * SEQ;
  const u32* wt = (const u32*)(ws + WS_WT) + (size_t)kd * SEQ;
  f32x2* lds = (f32x2*)smem;
  f32x2 v[8];
#pragma unroll
  for (int j = 0; j < 8; ++j) v[j] = col[tid + 256 * j];
  u32 wv[8];
  {
    const int hi = lane >> 3, lo = lane & 7;
#pragma unroll
    for (int k2 = 0; k2 < 8; ++k2) wv[k2] = wt[wave + 4 * (hi + 8 * lo + 64 * k2)];
  }
  Tw512 tw;
  make_tw512(tw, lane);
  f32x2 t4[2][3];
#pragma unroll
  for (int h = 0; h < 2; ++h)
#pragma unroll
    for (int k = 1; k < 4; ++k) t4[h][k - 1] = twid((float)(((tid + 256 * h) * k) & 2047) * (1.0f / 2048.0f));
#pragma unroll
  for (int h = 0; h < 2; ++h) {
    f32x2 y0, y1, y2, y3;
    dft4<-1>(v[h], v[2 + h], v[4 + h], v[6 + h], y0, y1, y2, y3);
    lds[0 * PS + tid + 256 * h] = y0;
    lds[1 * PS + tid + 256 * h] = cmul(y1, t4[h][0]);
    lds[2 * PS + tid + 256 * h] = cmul(y2, t4[h][1]);
    lds[3 * PS + tid + 256 * h] = cmul(y3, t4[h][2]);
  }
  __syncthreads();
  f32x2* buf = lds + wave * PS;
#pragma unroll
  for (int j = 0; j < 8; ++j) v[j] = buf[lane + 64 * j];
  fft512_fwd(v, buf, tw, lane);
#pragma unroll
  for (int k2 = 0; k2 < 8; ++k2) {
    f32x2 w;
    w.x = __uint_as_float(wv[k2] << 16);
    w.y = __uint_as_float(wv[k2] & 0xffff0000u);
    v[k2] = cmul(v[k2], w);
  }
  fft512_inv(v, buf, tw, lane);
  wave_sync();
#pragma unroll
  for (int j = 0; j < 8; ++j) buf[lane + 64 * j] = v[j];
  __syncthreads();
#pragma unroll
  for (int h = 0; h < 2; ++h) {
    f32x2 y0 = lds[0 * PS + tid + 256 * h];
    f32x2 y1 = cmulc(lds[1 * PS + tid + 256 * h], t4[h][0]);
    f32x2 y2 = cmulc(lds[2 * PS + tid + 256 * h], t4[h][1]);
    f32x2 y3 = cmulc(lds[3 * PS + tid + 256 * h], t4[h][2]);
    f32x2 o0, o1, o2, o3;
    dft4<1>(y0, y1, y2, y3, o0, o1, o2, o3);
    col[tid + 256 * h] = o0;
    col[tid + 256 * h + 512] = o1;
    col[tid + 256 * h + 1024] = o2;
    col[tid + 256 * h + 1536] = o3;
  }
}

__global__ __launch_bounds__(256, 2) void k_l2(char* __restrict__ ws) {
  extern __shared__ __attribute__((aligned(16))) char smem[];
  if (blockIdx.x < 256) g2_body(ws, smem, blockIdx.x);
  else k2_body(ws, smem, blockIdx.x - 256);
}

__global__ __launch_bounds__(256) void k_l3(const char* __restrict__ ws, u16* __restrict__ out) {
  __shared__ __attribute__((aligned(16))) f32x2 lds[4 * PS];
  const int tid = threadIdx.x, lane = tid & 63;
  const int wave = __builtin_amdgcn_readfirstlane(tid >> 6);
  const int s0 = blockIdx.x * 8;
  const f32x2* zt = (const f32x2*)(ws + WS_YT);
  f32x4 z[5];
#pragma unroll
  for (int r = 0; r < 5; ++r) {
    const int i = tid + 256 * r;
    if (i < DF * 4) z[r] = *(const f32x4*)(zt + (size_t)(i >> 2) * SEQ + s0 + 2 * (i & 3));
  }
  const int row = s0 + 2 * wave;
  const u16* bb = (const u16*)(ws + WS_B) + (size_t)row * DM;
  u16 b0[8], b1[8];
#pragma unroll
  for (int j = 0; j < 8; ++j) { b0[j] = bb[lane + 64 * j]; b1[j] = bb[DM + lane + 64 * j]; }
  const float g0 = ((const float*)(ws + WS_G))[row], g1 = ((const float*)(ws + WS_G))[row + 1];
  Tw512 tw;
  make_tw512(tw, lane);
#pragma unroll
  for (int r = 0; r < 5; ++r) {
    const int i = tid + 256 * r;
    if (i < DF * 4) {
      const int kd = i >> 2, p = i & 3;
      f32x4 q = z[r];
      if (kd == 0 || kd == 256) { q.y = 0.f; q.w = 0.f; }
      f32x2 c1, c2;
      c1.x = q.x - q.w; c1.y = q.y + q.z;
      c2.x = q.x + q.w; c2.y = q.z - q.y;
      lds[p * PS + sslot(kd)] = c1;
      if (kd != 0 && kd != 256) lds[p * PS + sslot(512 - kd)] = c2;
    }
  }
  __syncthreads();
  f32x2* buf = lds + wave * PS;
  f32x2 v[8];
  {
    const int hi = lane >> 3, lo = lane & 7;
#pragma unroll
    for (int k2 = 0; k2 < 8; ++k2) v[k2] = buf[sslot(hi + 8 * lo + 64 * k2)];
  }
  fft512_inv(v, buf, tw, lane);
  const float sc = 1.0f / (2048.0f * 512.0f);
  const float og0 = bfr(1.0f - g0), og1 = bfr(1.0f - g1);
  u16* o = out + (size_t)row * DM;
#pragma unroll
  for (int j = 0; j < 8; ++j) {
    const float a0 = bfr(v[j].x * sc), a1 = bfr(v[j].y * sc);
    o[lane + 64 * j] = f2bf(bfr(g0 * a0) + bfr(og0 * bf2f(b0[j])));
    o[DM + lane + 64 * j] = f2bf(bfr(g1 * a1) + bfr(og1 * bf2f(b1[j])));
  }
}

extern "C" void kernel_launch(void* const* d_in, const int* in_sizes, int n_in, void* d_out, int out_size,
                              void* d_ws, size_t ws_size, hipStream_t stream) {
  const u16* x = (const u16*)d_in[0];
  const u16* wr = (const u16*)d_in[1];
  const u16* wi = (const u16*)d_in[2];
  const u16* wg = (const u16*)d_in[4];
  const u16* wu = (const u16*)d_in[5];
  const u16* wd = (const u16*)d_in[6];
  const u16* gw = (const u16*)d_in[7];
  char* ws = (char*)d_ws;
  (void)hipFuncSetAttribute((const void*)k_l1, hipFuncAttributeMaxDynamicSharedMemorySize, L1_LDS);
  (void)hipFuncSetAttribute((const void*)k_l2, hipFuncAttributeMaxDynamicSharedMemorySize, L2_LDS);
  k_l1<<<256, 512, L1_LDS, stream>>>(x, wr, wi, wg, wu, wd, gw, ws);
  k_l2<<<256 + DF, 256, L2_LDS, stream>>>(ws);
  k_l3<<<256, 256, 0, stream>>>(ws, (u16*)d_out);
}
```

```cpp
#include <hip/hip_runtime.h>
#include <hip/hip_bf16.h>
#include <stdint.h>

#if defined(__HIP_DEVICE_COMPILE__)
#define NOPK __attribute__((target("no-packed-fp32-ops")))
#else
#define NOPK
#endif
#define DEVI NOPK __device__ __forceinline__
#define TS(i)

typedef __attribute__((ext_vector_type(8))) short bf16x8;
typedef __attribute__((ext_vector_type(4))) short bf16x4;
typedef __attribute__((ext_vector_type(4))) float f32x4;
typedef __attribute__((ext_vector_type(2))) float f32x2;
typedef unsigned short u16;
typedef unsigned int u32;
typedef __attribute__((ext_vector_type(4))) unsigned int u32x4;
typedef __attribute__((ext_vector_type(2))) unsigned int u32x2;
typedef __attribute__((address_space(3))) bf16x4* lds_b4_ptr;
#define TIDX ((int)__builtin_amdgcn_workitem_id_x())
#define BIDX ((int)__builtin_amdgcn_workgroup_id_x())
#define U2F(x) __builtin_bit_cast(float, (u32)(x))
#define SYNC() do { __builtin_amdgcn_fence(__ATOMIC_RELEASE, "workgroup"); __builtin_amdgcn_s_barrier(); \
                    __builtin_amdgcn_fence(__ATOMIC_ACQUIRE, "workgroup"); } while (0)
DEVI float fexp(float x) { return __builtin_amdgcn_exp2f(x * 1.44269504088896340736f); }
DEVI float shfl_xor(float v, int m, int lane) {
  return __builtin_bit_cast(float, __builtin_amdgcn_ds_bpermute((lane ^ m) << 2, __builtin_bit_cast(int, v)));
}

constexpr int SEQ = 2048, DM = 512, DF = 257, FF = 1376;
constexpr int KS32 = FF / 32;

constexpr size_t WS_YT = 0;
constexpr size_t WS_WT = WS_YT + (size_t)DF * SEQ * 8;
constexpr size_t WS_H = WS_WT + (size_t)DF * SEQ * 4;
constexpr size_t WS_WD = WS_H + (size_t)SEQ * FF * 2;
constexpr size_t WS_B = WS_WD + (size_t)FF * DM * 2;
constexpr size_t WS_G = WS_B + (size_t)SEQ * DM * 2;

DEVI float bf2f(u16 v) { return U2F(((u32)v) << 16); }
DEVI u16 f2bf(float f) {
  __bf16 b = (__bf16)f;
  return __builtin_bit_cast(u16, b);
}
DEVI float bfr(float f) { return bf2f(f2bf(f)); }

DEVI f32x2 cmul(f32x2 a, f32x2 b) {
  f32x2 r;
  r.x = a.x * b.x - a.y * b.y;
  r.y = a.x * b.y + a.y * b.x;
  return r;
}
DEVI f32x2 cmulc(f32x2 a, f32x2 b) {
  f32x2 r;
  r.x = a.x * b.x + a.y * b.y;
  r.y = a.y * b.x - a.x * b.y;
  return r;
}
DEVI f32x2 twid(float frac) {
  f32x2 r;
  r.x = __builtin_amdgcn_cosf(frac);
  r.y = -__builtin_amdgcn_sinf(frac);
  return r;
}

template <int SGN>
DEVI f32x2 mul_i(f32x2 z) {
  f32x2 r;
  if (SGN < 0) { r.x = z.y; r.y = -z.x; } else { r.x = -z.y; r.y = z.x; }
  return r;
}
template <int SGN>
DEVI void dft4(f32x2 b0, f32x2 b1, f32x2 b2, f32x2 b3, f32x2& y0, f32x2& y1, f32x2& y2,
                                     f32x2& y3) {
  f32x2 c0 = b0 + b2, c1 = b0 - b2, c2 = b1 + b3, c3 = mul_i<SGN>(b1 - b3);
  y0 = c0 + c2; y2 = c0 - c2; y1 = c1 + c3; y3 = c1 - c3;
}
template <int SGN>
DEVI void dft8(f32x2 (&v)[8]) {
  const float R = 0.70710678118654752440f;
  f32x2 a0 = v[0] + v[4], a4 = v[0] - v[4];
  f32x2 a1 = v[1] + v[5], a5 = v[1] - v[5];
  f32x2 a2 = v[2] + v[6], a6 = v[2] - v[6];
  f32x2 a3 = v[3] + v[7], a7 = v[3] - v[7];
  f32x2 t5, t7;
  if (SGN < 0) {
    t5.x = (a5.x + a5.y) * R; t5.y = (a5.y - a5.x) * R;
    t7.x = (a7.y - a7.x) * R; t7.y = -(a7.x + a7.y) * R;
  } else {
    t5.x = (a5.x - a5.y) * R; t5.y = (a5.x + a5.y) * R;
    t7.x = -(a7.x + a7.y) * R; t7.y = (a7.x - a7.y) * R;
  }
  f32x2 t6 = mul_i<SGN>(a6);
  dft4<SGN>(a0, a1, a2, a3, v[0], v[2], v[4], v[6]);
  dft4<SGN>(a4, t5, t6, t7, v[1], v[3], v[5], v[7]);
}

DEVI void wave_sync() {
  asm volatile("s_waitcnt lgkmcnt(0)" ::: "memory");
  __builtin_amdgcn_wave_barrier();
}

struct Tw512 {
  f32x2 a[7];
  f32x2 b[7];
};
DEVI void make_tw512(Tw512& t, int lane) {
#pragma unroll
  for (int k = 1; k < 8; ++k) {
    t.a[k - 1] = twid((float)((lane * k) & 511) * (1.0f / 512.0f));
    t.b[k - 1] = twid((float)(((lane & 7) * k) & 63) * (1.0f / 64.0f));
  }

}

DEVI void fft512_fwd(f32x2 (&v)[8], f32x2* buf, const Tw512& tw, int lane) {
  const int hi = lane >> 3, lo = lane & 7;
  dft8<-1>(v);
#pragma unroll
  for (int k = 1; k < 8; ++k) v[k] = cmul(v[k], tw.a[k - 1]);
  wave_sync();
#pragma unroll
  for (int k = 0; k < 8; ++k) buf[72 * k + lane] = v[k];
  wave_sync();
#pragma unroll
  for (int n = 0; n < 8; ++n) v[n] = buf[72 * hi + 8 * n + lo];
  dft8<-1>(v);
#pragma unroll
  for (int k = 1; k < 8; ++k) v[k] = cmul(v[k], tw.b[k - 1]);
  wave_sync();
#pragma unroll
  for (int k = 0; k < 8; ++k) buf[72 * hi + 9 * k + lo] = v[k];
  wave_sync();
#pragma unroll
  for (int n = 0; n < 8; ++n) v[n] = buf[72 * hi + 9 * lo + n];
  dft8<-1>(v);
}
DEVI void fft512_inv(f32x2 (&v)[8], f32x2* buf, const Tw512& tw, int lane) {
  const int hi = lane >> 3, lo = lane & 7;
  dft8<1>(v);
  wave_sync();
#pragma unroll
  for (int n = 0; n < 8; ++n) buf[72 * hi + 9 * lo + n] = v[n];
  wave_sync();
#pragma unroll
  for (int k = 0; k < 8; ++k) v[k] = buf[72 * hi + 9 * k + lo];
#pragma unroll
  for (int k = 1; k < 8; ++k) v[k] = cmulc(v[k], tw.b[k - 1]);
  dft8<1>(v);
  wave_sync();
#pragma unroll
  for (int n = 0; n < 8; ++n) buf[72 * hi + 8 * n + lo] = v[n];
  wave_sync();
#pragma unroll
  for (int k = 0; k < 8; ++k) v[k] = buf[72 * k + lane];
#pragma unroll
  for (int k = 1; k < 8; ++k) v[k] = cmulc(v[k], tw.a[k - 1]);
  dft8<1>(v);
}

DEVI int sslot(int k) { return k + 4 * (k >> 5); }
constexpr int PS = 584;

#define PREFETCH 0
constexpr int G1_NS = 3;
constexpr int G1_X_OFF = G1_NS * 24576;
constexpr int AX_OFF = G1_X_OFF + G1_NS * 16384;
constexpr int AX_W_OFF = AX_OFF + 4 * PS * 8;
constexpr int AX_WD_OFF = AX_W_OFF + 10240;
constexpr int AX_GW_OFF = AX_WD_OFF;
constexpr int L1_CNT_OFF = AX_WD_OFF + 8192;
constexpr int L1_LDS = L1_CNT_OFF + 256;

#define WAIT_V(n) asm volatile("s_waitcnt vmcnt(" #n ")" ::: "memory")
#define WAIT_L(n) asm volatile("s_waitcnt lgkmcnt(" #n ")" ::: "memory")
#define RAW_BAR() asm volatile("s_barrier" ::: "memory")

typedef __amdgpu_buffer_rsrc_t rsrc_t;
typedef __attribute__((ext_vector_type(4))) int i32x4;
DEVI void dma16(const void* gsrc, u32 lds_dst) {
  u32 keep;
  asm volatile("s_mov_b32 %0, m0\n\ts_mov_b32 m0, %2\n\ts_nop 0\n\tglobal_load_lds_dwordx4 %1, off\n\ts_mov_b32 m0, %0"
               : "=&s"(keep) : "v"(gsrc), "s"(lds_dst) : "memory");
}
DEVI void dma16b(i32x4 rs, u32 voff, u32 soff, u32 lds_dst) {
  u32 keep;
  asm volatile("s_mov_b32 %0, m0\n\ts_mov_b32 m0, %4\n\ts_nop 0\n\tbuffer_load_dwordx4 %1, %2, %3 offen lds\n\ts_mov_b32 m0, %0"
               : "=&s"(keep) : "v"(voff), "s"(rs), "s"(soff), "s"(lds_dst) : "memory");
}
DEVI i32x4 mkrs(const void* p, u32 bytes) {
  const unsigned long long a = (unsigned long long)p;
  i32x4 r;
  r.x = __builtin_amdgcn_readfirstlane((int)(u32)a);
  r.y = __builtin_amdgcn_readfirstlane((int)(u32)(a >> 32) & 0xffff);
  r.z = (int)bytes;
  r.w = 0x00020000;
  return r;
}
DEVI rsrc_t mkrsrc(const void* p) { return __builtin_amdgcn_make_buffer_rsrc((void*)p, 0, 0x7fffffff, 0x00020000); }
DEVI void st16(rsrc_t r, u32 off, u32x4 v) { __builtin_amdgcn_raw_buffer_store_b128(v, r, off, 0, 16); }
DEVI u32 pack2(float a, float b) { return (u32)f2bf(a) | ((u32)f2bf(b) << 16); }

typedef __attribute__((address_space(3))) u32* lds_u32_ptr;
DEVI void lds_signal(u32 addr, int lane) {
  unsigned long long keep;
  asm volatile("s_mov_b64 %0, exec\n\ts_mov_b64 exec, 1\n\tds_add_u32 %1, %2\n\ts_mov_b64 exec, %0"
               : "=&s"(keep) : "v"(addr), "v"(1u) : "memory");
}
DEVI void lds_wait1(u32 addr, u32 need) {
  while (true) {
    u32 v;
    asm volatile("ds_read_b32 %0, %1\n\ts_waitcnt lgkmcnt(0)" : "=v"(v) : "v"(addr) : "memory");
    if ((u32)__builtin_amdgcn_readfirstlane(v) >= need) break;
    __builtin_amdgcn_s_sleep(1);
  }
}
DEVI void lds_wait2(u32 addr, u32 need0, u32 need1) {
  while (true) {
    u32x2 v;
    asm volatile("ds_read_b64 %0, %1\n\ts_waitcnt lgkmcnt(0)" : "=v"(v) : "v"(addr) : "memory");
    const u32 a = __builtin_amdgcn_readfirstlane(v.x), b = __builtin_amdgcn_readfirstlane(v.y);
    if (a >= need0 && b >= need1) break;
    __builtin_amdgcn_s_sleep(1);
  }
}

DEVI void aux_body(const u16* __restrict__ x, const u16* __restrict__ wr, const u16* __restrict__ wi,
                   const u16* __restrict__ wd, const u16* __restrict__ gw, char* __restrict__ ws, char* smem, int a,
                   int wave, int lane) {
  constexpr int TSK = 0;
  const u32 lds0 = (u32)(size_t)(__attribute__((address_space(3))) char*)smem;
  const rsrc_t rws = mkrsrc(ws);
  const int s0 = a * 8;
  const int tid = wave * 64 + lane;
  f32x2* aux = (f32x2*)(smem + AX_OFF);
  {
    const int p = wave;
    Tw512 tw;
    make_tw512(tw, lane);
    WAIT_V(0);
    TS(1);
    const u16* xa = (const u16*)(aux + p * PS);
    const u16* gl = (const u16*)(smem + AX_GW_OFF + p * 1024);
    u16 ra[8], rb[8], rg[8];
#pragma unroll
    for (int j = 0; j < 8; ++j) {
      ra[j] = xa[lane + 64 * j];
      rb[j] = xa[DM + lane + 64 * j];
      rg[j] = gl[lane + 64 * j];
    }
    f32x2 v[8];
    float sa = 0.f, sb = 0.f;
#pragma unroll
    for (int j = 0; j < 8; ++j) {
      v[j].x = bf2f(ra[j]);
      v[j].y = bf2f(rb[j]);
      float w = bf2f(rg[j]);
      sa += bfr(v[j].x * w);
      sb += bfr(v[j].y * w);
    }
    {
      WAIT_L(0);
      const char* pr = (const char*)wr + (size_t)a * 4112;
      const char* pi = (const char*)wi + (size_t)a * 4112;
      dma16(pr + p * 1024 + lane * 16, lds0 + AX_W_OFF + p * 1024);
      dma16(pi + p * 1024 + lane * 16, lds0 + AX_W_OFF + 5120 + p * 1024);
      if (p < 2) dma16((p ? pi : pr) + (lane == 0 ? 4096 : 0), lds0 + AX_W_OFF + p * 5120 + 4096);
      const char* pd = (const char*)wd + (size_t)(a < 172 ? a : 0) * 8192;
      dma16(pd + p * 1024 + lane * 16, lds0 + AX_WD_OFF + p * 1024);
      dma16(pd + 4096 + p * 1024 + lane * 16, lds0 + AX_WD_OFF + 4096 + p * 1024);
    }
#pragma unroll
    for (int o = 32; o > 0; o >>= 1) {
      sa += shfl_xor(sa, o, lane);
      sb += shfl_xor(sb, o, lane);
    }
    if (lane < 2) {
      float s = bfr(lane ? sb : sa);
      float gg = bfr(1.0f / (1.0f + fexp(-s)));
      ((float*)(ws + WS_G))[s0 + 2 * p + lane] = gg;
    }
    f32x2* buf = aux + p * PS;
    fft512_fwd(v, buf, tw, lane);
    wave_sync();
    const int hi = lane >> 3, lo = lane & 7;
#pragma unroll
    for (int k2 = 0; k2 < 8; ++k2) buf[sslot(hi + 8 * lo + 64 * k2)] = v[k2];
  }
  WAIT_L(0);
  lds_signal(lds0 + L1_CNT_OFF + 42 * 4, lane);
  WAIT_V(0);
  lds_signal(lds0 + L1_CNT_OFF + 43 * 4, lane);
  TS(2);
  lds_wait1(lds0 + L1_CNT_OFF + 42 * 4, 4);
#pragma unroll
  for (int r = 0; r < 4; ++r) {
    const int i = tid + 256 * r;
    const int kd = i >> 2, p = i & 3;
    const f32x2 z1 = aux[p * PS + sslot(kd)];
    const f32x2 z2 = aux[p * PS + sslot(kd ? 512 - kd : 256)];
    f32x4 o;
    if (kd) {
      o.x = 0.5f * (z1.x + z2.x);
      o.y = 0.5f * (z1.y - z2.y);
      o.z = 0.5f * (z1.y + z2.y);
      o.w = 0.5f * (z2.x - z1.x);
    } else {
      o.x = z1.x; o.y = z2.x; o.z = z1.y; o.w = z2.y;
    }
    st16(rws, (u32)WS_YT + (u32)(kd * SEQ + s0 + 2 * p) * 8, __builtin_bit_cast(u32x4, o));
  }
  TS(3);
  lds_wait1(lds0 + L1_CNT_OFF + 43 * 4, 4);
  {
    const u16* lr = (const u16*)(smem + AX_W_OFF);
    const u16* li = (const u16*)(smem + AX_W_OFF + 5120);
#pragma unroll
    for (int r = 0; r < 3; ++r) {
      const int i = tid + 256 * r;
      if (i < DF * 2) {
        const int kd = i >> 1, h = i & 1;
        u32x4 o;
#pragma unroll
        for (int q = 0; q < 4; ++q) o[q] = (u32)lr[(h * 4 + q) * DF + kd] | ((u32)li[(h * 4 + q) * DF + kd] << 16);
        st16(rws, (u32)WS_WT + (u32)(kd * SEQ + s0 + h * 4) * 4, o);
      }
    }
  }
  if (a < 172) {
    const u16* ld = (const u16*)(smem + AX_WD_OFF);
#pragma unroll
    for (int r = 0; r < 2; ++r) {
      const int n = tid + 256 * r;
      u32x4 o;
#pragma unroll
      for (int q = 0; q < 4; ++q) o[q] = (u32)ld[(2 * q) * DM + n] | ((u32)ld[(2 * q + 1) * DM + n] << 16);
      const int ks = a >> 2, kq = a & 3;
      st16(rws, (u32)WS_WD + (u32)(((ks * 32 + (n >> 4)) * 64 + (n & 15) + 16 * kq) * 16), o);
    }
  }
  TS(4);
}

NOPK __global__ __launch_bounds__(1024, 4) void k_l1(const u16* __restrict__ x, const u16* __restrict__ wr,
                                                const u16* __restrict__ wi, const u16* __restrict__ wg,
                                                const u16* __restrict__ wu, const u16* __restrict__ wd,
                                                const u16* __restrict__ gw, char* __restrict__ ws) {
  extern __shared__ __attribute__((aligned(16))) char smem[];
  const int bid = BIDX;
  const int tid = TIDX, lane = tid & 63;
  const int wave = __builtin_amdgcn_readfirstlane(tid >> 6);
  constexpr int TSK = 0;
  TS(0);
  const u32 lds0 = (u32)(size_t)(__attribute__((address_space(3))) char*)smem;
  const u32 cnt = lds0 + L1_CNT_OFF;
  if (wave == 0) { ((u32*)(smem + L1_CNT_OFF))[lane] = 0; WAIT_L(0); }
  RAW_BAR();
  const int xcd = bid & 7, idx = bid >> 3;
  const int T = xcd * 30 + (idx < 30 ? idx : 0);
  const int fg = T / 80, rem = T - fg * 80;
  const int mt_ = rem / 5, fi = rem - mt_ * 5;
  const int m0 = mt_ * 128, f0 = (fg * 5 + fi) * 96;
  if (wave >= 12) {
    const int w = wave - 12;
    u32 d0, d1, d2, d3, d4, d5;
    if (idx < 30 && w > 0) {
      const int part = mt_ % 6;
      auto waddr = [&](int it) NOPK __attribute__((always_inline)) {
        it = it > 65 ? 65 : it;
        const int r = it / 3, sec = it - r * 3;
        int wrow = 22 * part + r;
        wrow = wrow > 127 ? 127 : wrow;
        int f = f0 + sec * 32;
        f = f > FF - 2 ? FF - 2 : f;
        return ((size_t)(wrow + 128 * w) * FF + f) * 2;
      };
      auto xaddr = [&](int it) NOPK __attribute__((always_inline)) {
        int xrow = 26 * fi + (it >> 2);
        xrow = xrow > 127 ? 127 : xrow;
        return (const char*)x + (size_t)(m0 + xrow) * 1024 + (it & 3) * 64 + w * 256;
      };
      const size_t wo0 = waddr(lane), wo1 = waddr(64 + (lane & 1));
      asm volatile("global_load_dword %0, %1, off" : "=v"(d0) : "v"((const char*)wg + wo0) : "memory");
      asm volatile("global_load_dword %0, %1, off" : "=v"(d1) : "v"((const char*)wu + wo0) : "memory");
      asm volatile("global_load_dword %0, %1, off" : "=v"(d2) : "v"(xaddr(lane)) : "memory");
      asm volatile("global_load_dword %0, %1, off" : "=v"(d3) : "v"(xaddr(64 + lane)) : "memory");
      asm volatile("global_load_dword %0, %1, off" : "=v"(d4) : "v"((const char*)wg + wo1) : "memory");
      asm volatile("global_load_dword %0, %1, off" : "=v"(d5) : "v"((const char*)wu + wo1) : "memory");
    }
    const char* px = (const char*)x + (size_t)(bid * 8 + 2 * w) * 1024;
    dma16(px + lane * 16, lds0 + AX_OFF + w * (PS * 8));
    dma16(px + 1024 + lane * 16, lds0 + AX_OFF + w * (PS * 8) + 1024);
    dma16((const char*)gw + lane * 16, lds0 + AX_GW_OFF + w * 1024);
    WAIT_V(3);
    asm volatile("" ::"v"(d0), "v"(d1), "v"(d2), "v"(d3), "v"(d4), "v"(d5));
    lds_signal(cnt + (32 + w) * 4, lane);
    aux_body(x, wr, wi, wd, gw, ws, smem, bid, w, lane);
    return;
  }
  if (idx >= 30) return;

  {
    const int q4 = wave >= 8 ? wave - 8 : (wave >> 1);
    const i32x4 rx = mkrs(x, SEQ * DM * 2), rg = mkrs(wg, DM * FF * 2), ru = mkrs(wu, DM * FF * 2);
    u32 vx, vw[3];
    {
      const int row = lane >> 3;
      const int c = (lane & 7) ^ row;
      vx = (u32)((m0 + q4 * 32 + row) * DM + c * 8) * 2;
#pragma unroll
      for (int j = 0; j < 3; ++j) {
        const int q = 64 * j + lane;
        const int k = q / 12, cp = q - k * 12;
        const int c2 = cp ^ (((k >> 3) & 1) << 1);
        vw[j] = (u32)((q4 * 16 + k) * FF + f0 + c2 * 8) * 2;
      }
    }
    auto issue = [&](int s, int lo, int hi) NOPK __attribute__((always_inline)) {
      const int slot = s % G1_NS;
      const u32 wdst = lds0 + slot * 24576 + q4 * 3072, xdst = lds0 + G1_X_OFF + slot * 16384 + q4 * 4096;
      const u32 sw = s * (64 * FF * 2);
#pragma unroll
      for (int n = 0; n < 10; ++n) {
        if (n >= lo && n < hi) {
          if (n < 3) dma16b(rg, vw[n], sw, wdst + n * 1024);
          else if (n < 7) dma16b(rx, vx, s * 128 + (n - 3) * 8192, xdst + (n - 3) * 1024);
          else dma16b(ru, vw[n - 7], sw, wdst + 12288 + (n - 7) * 1024);
        }
      }
    };
    if (wave >= 8) {
      issue(1, 0, 10);
      WAIT_V(0);
      lds_signal(cnt + 1 * 4, lane);
      lds_wait1(cnt + 33 * 4, 1);
      issue(2, 0, 10);
#pragma unroll 1
      for (int s = 3; s < 8; ++s) {
        if ((s & 1) == 0) lds_wait1(cnt + (32 + (s >> 1)) * 4, 1);
        lds_wait1(cnt + (16 + s - 3) * 4, 8);
        issue(s, 0, 3);
        WAIT_V(3);
        lds_signal(cnt + (s - 1) * 4, lane);
        issue(s, 3, 10);
      }
      WAIT_V(0);
      lds_signal(cnt + 7 * 4, lane);
      return;
    }
    if (wave & 1) issue(0, 5, 10); else issue(0, 0, 5);
    WAIT_V(0);
    lds_signal(cnt, lane);
  }

  const int wm = wave >> 1, wf = wave & 1;
  const int l15 = lane & 15, lq = lane >> 4;
  int xoff[2][2];
#pragma unroll
  for (int mt = 0; mt < 2; ++mt)
#pragma unroll
    for (int kk = 0; kk < 2; ++kk) {
      const int row = wm * 32 + mt * 16 + l15;
      xoff[mt][kk] = G1_X_OFF + row * 128 + (((kk * 4 + lq) ^ (row & 7)) << 4);
    }
  const int wq = l15 >> 2, wp = l15 & 3;
  int woff[3];
#pragma unroll
  for (int ft = 0; ft < 3; ++ft)
    woff[ft] = (8 * lq + wq) * 192 + (((2 * (wf * 3 + ft) + (wp >> 1)) ^ (2 * (lq & 1))) << 4) + (wp & 1) * 8;

  f32x4 accg[3][2], accu[3][2];
#pragma unroll
  for (int a = 0; a < 3; ++a)
#pragma unroll
    for (int b = 0; b < 2; ++b) { accg[a][b] = (f32x4)0.f; accu[a][b] = (f32x4)0.f; }

  bf16x8 xa[2], xb[2], wgf[3], wuf[3];
  auto rdx = [&](bf16x8(&xf)[2], int xs, int kk) NOPK __attribute__((always_inline)) {
#pragma unroll
    for (int mt = 0; mt < 2; ++mt) xf[mt] = *(const bf16x8*)(smem + xs + xoff[mt][kk]);
  };
  auto rdw = [&](bf16x8(&wfr)[3], int wsl, int kk, int mat) NOPK __attribute__((always_inline)) {
#pragma unroll
    for (int n = 0; n < 3; ++n) {
      const char* base = smem + (wsl + woff[n]) + kk * 6144 + mat * 12288;
      bf16x4 lo = __builtin_amdgcn_ds_read_tr16_b64_v4i16((lds_b4_ptr)(base));
      bf16x4 hi = __builtin_amdgcn_ds_read_tr16_b64_v4i16((lds_b4_ptr)(base + 768));
      wfr[n] = __builtin_shufflevector(lo, hi, 0, 1, 2, 3, 4, 5, 6, 7);
    }
  };
  auto mma = [&](f32x4(&acc)[3][2], bf16x8(&xf)[2], bf16x8(&wfr)[3]) NOPK __attribute__((always_inline)) {
#pragma unroll
    for (int ft = 0; ft < 3; ++ft)
#pragma unroll
      for (int mt = 0; mt < 2; ++mt)
        acc[ft][mt] = __builtin_amdgcn_mfma_f32_16x16x32_bf16(wfr[ft], xf[mt], acc[ft][mt], 0, 0, 0);
  };
  auto interleave = [&]() NOPK __attribute__((always_inline)) {
#pragma unroll
    for (int i = 0; i < 6; ++i) {
      __builtin_amdgcn_sched_group_barrier(0x008, 1, 0);
      __builtin_amdgcn_sched_group_barrier(0x100, 2, 0);
    }
  };
  const volatile u32* cv = (const volatile u32*)(smem + L1_CNT_OFF);

  TS(1);
  lds_wait1(cnt, 8);
  TS(4);
  rdx(xa, 0, 0);
  rdw(wgf, 0, 0, 0);
#define SB() __builtin_amdgcn_sched_barrier(0)
  int slot = 0;
#pragma unroll 1
  for (int S = 0; S < 8; ++S) {
    const int wsl = slot * 24576, xs = slot * 16384;
    slot = slot == G1_NS - 1 ? 0 : slot + 1;
    const int wsn = slot * 24576, xn = slot * 16384;
    SB(); rdw(wuf, wsl, 0, 1); mma(accg, xa, wgf); interleave();
    SB(); rdw(wgf, wsl, 1, 0); rdx(xb, xs, 1); mma(accu, xa, wuf); interleave();
    SB(); rdw(wuf, wsl, 1, 1); const u32 fv = cv[S + 1]; mma(accg, xb, wgf); interleave();
    SB();
    WAIT_L(0);
    lds_signal(cnt + (16 + S) * 4, lane);
    if (S < 7) {
      if ((u32)__builtin_amdgcn_readfirstlane(fv) < 4) lds_wait1(cnt + (S + 1) * 4, 4);
    }
    SB();
    rdw(wgf, wsn, 0, 0); rdx(xa, xn, 0);
    mma(accu, xb, wuf);
    interleave();
  }
  SB();
  TS(5);
  const rsrc_t rws = mkrsrc(ws);
  const int lane2 = __builtin_amdgcn_mbcnt_hi(~0u, __builtin_amdgcn_mbcnt_lo(~0u, 0u));
  const int e15 = lane2 & 15, eq = lane2 >> 4;
#pragma unroll
  for (int ft = 0; ft < 3; ++ft) {
    u32 pk[2][2];
#pragma unroll
    for (int mt = 0; mt < 2; ++mt) {
      float o[4];
#pragma unroll
      for (int r = 0; r < 4; ++r) {
        const float g = bfr(accg[ft][mt][r]);
        const float sl = bfr(g * __builtin_amdgcn_rcpf(1.0f + fexp(-g)));
        const float u = bfr(accu[ft][mt][r]);
        o[r] = sl * u;
      }
      pk[mt][0] = pack2(o[0], o[1]);
      pk[mt][1] = pack2(o[2], o[3]);
    }
    auto s0_ = __builtin_amdgcn_permlane16_swap(pk[0][0], pk[1][0], false, false);
    auto s1_ = __builtin_amdgcn_permlane16_swap(pk[0][1], pk[1][1], false, false);
    u32x4 ov;
    ov.x = s0_[0]; ov.y = s1_[0]; ov.z = s0_[1]; ov.w = s1_[1];
    const int f = f0 + (wf * 3 + ft) * 16 + 8 * (eq >> 1);
    const int m = m0 + wm * 32 + (eq & 1) * 16 + e15;
    if (f < FF)
      st16(rws, (u32)WS_H + (u32)(((m >> 4) * KS32 + (f >> 5)) * 1024 + ((m & 15) + 16 * ((f & 31) >> 3)) * 16), ov);
  }
  TS(6);
}

constexpr int L2_LDS = 4 * PS * 8 + 32768;
constexpr int G2_DEPTH = 4;

DEVI void g2_body(char* __restrict__ ws, char* smem, int bid) {
  const int tid = TIDX, lane = tid & 63;
  const int wave = __builtin_amdgcn_readfirstlane(tid >> 6);
  constexpr int TSK = 1;
  TS(0);
  const int xcd = bid & 7, idx = bid >> 3;
  const int mt64 = xcd * 4 + (idx >> 3), nt64 = idx & 7;
  const char* hb = ws + WS_H + (size_t)(mt64 * 4) * KS32 * 1024 + lane * 16;
  const char* wb = ws + WS_WD + (size_t)(nt64 * 4) * 1024 + lane * 16;
  f32x4 acc[4][4];
#pragma unroll
  for (int a = 0; a < 4; ++a)
#pragma unroll
    for (int b = 0; b < 4; ++b) acc[a][b] = (f32x4)0.f;
  constexpr int NS = G2_DEPTH + 1;
  bf16x8 hf[NS][4], wf[NS][4];
  auto ld = [&](int slot, int i) NOPK __attribute__((always_inline)) {
    int ks = wave + 4 * i;
    ks = ks > KS32 - 1 ? KS32 - 1 : ks;
#pragma unroll
    for (int j = 0; j < 4; ++j) {
      hf[slot][j] = *(const bf16x8*)(hb + ((size_t)j * KS32 + ks) * 1024);
      wf[slot][j] = *(const bf16x8*)(wb + ((size_t)ks * 32 + j) * 1024);
    }
  };
#pragma unroll
  for (int i = 0; i < G2_DEPTH; ++i) ld(i, i);
  __builtin_amdgcn_sched_barrier(0);
#pragma unroll
  for (int i = 0; i < 11; ++i) {
    if (i + G2_DEPTH < 11) ld((i + G2_DEPTH) % NS, i + G2_DEPTH);
    __builtin_amdgcn_sched_barrier(0);
    if (i < 10 || wave < 3) {
#pragma unroll
      for (int nt = 0; nt < 4; ++nt)
#pragma unroll
        for (int mt = 0; mt < 4; ++mt)
          acc[nt][mt] = __builtin_amdgcn_mfma_f32_16x16x32_bf16(wf[i % NS][nt], hf[i % NS][mt], acc[nt][mt], 0, 0, 0);
    }
    __builtin_amdgcn_sched_barrier(0);
  }
  TS(1);
  f32x4* red = (f32x4*)smem;
#pragma unroll
  for (int nt = 0; nt < 4; ++nt) {
    if (nt != wave) {
      const int src = wave < nt ? wave : wave - 1;
#pragma unroll
      for (int mt = 0; mt < 4; ++mt) red[((nt * 3 + src) * 4 + mt) * 64 + lane] = acc[nt][mt];
    }
  }
  SYNC();
  f32x4 fin[4];
#pragma unroll
  for (int nt = 0; nt < 4; ++nt)
    if (nt == wave) {
#pragma unroll
      for (int mt = 0; mt < 4; ++mt) fin[mt] = acc[nt][mt];
    }
  const rsrc_t rws = mkrsrc(ws);
  const int l15 = lane & 15, lq = lane >> 4;
#pragma unroll
  for (int mt = 0; mt < 4; ++mt)
#pragma unroll
    for (int s = 0; s < 3; ++s) fin[mt] += red[((wave * 3 + s) * 4 + mt) * 64 + lane];
#pragma unroll
  for (int pr = 0; pr < 2; ++pr) {
    auto s0_ = __builtin_amdgcn_permlane16_swap(pack2(fin[2 * pr][0], fin[2 * pr][1]),
                                                pack2(fin[2 * pr + 1][0], fin[2 * pr + 1][1]), false, false);
    auto s1_ = __builtin_amdgcn_permlane16_swap(pack2(fin[2 * pr][2], fin[2 * pr][3]),
                                                pack2(fin[2 * pr + 1][2], fin[2 * pr + 1][3]), false, false);
    u32x4 ov;
    ov.x = s0_[0]; ov.y = s1_[0]; ov.z = s0_[1]; ov.w = s1_[1];
    const int m = mt64 * 64 + (2 * pr + (lq & 1)) * 16 + l15;
    const int n = nt64 * 64 + wave * 16 + 8 * (lq >> 1);
    st16(rws, (u32)WS_B + (u32)(m * DM + n) * 2, ov);
  }
  TS(2);
}

DEVI f32x2 unpk(u32 w) {
  f32x2 r;
  r.x = U2F(w << 16);
  r.y = U2F(w & 0xffff0000u);
  return r;
}

DEVI void k2_body(char* __restrict__ ws, char* smem, int kd) {
  const int tid = TIDX, lane = tid & 63;
  const int wave = __builtin_amdgcn_readfirstlane(tid >> 6);
  constexpr int TSK = 1;
  TS(0);
  const f32x4* col = (const f32x4*)(ws + WS_YT + (size_t)kd * SEQ * 8);
  const u32* wt = (const u32*)(ws + WS_WT) + (size_t)kd * SEQ;
  f32x2* lds = (f32x2*)smem;
  f32x4 q[4];
#pragma unroll
  for (int n3 = 0; n3 < 4; ++n3) q[n3] = col[tid + 256 * n3];
  const int hi = lane >> 3, lo = lane & 7;
  const int kbase = wave + 4 * (hi + 8 * lo);
  u32 wv[8];
#pragma unroll
  for (int k2 = 0; k2 < 8; ++k2) wv[k2] = wt[kbase + 256 * k2];
  u32 wx[8];
  if (kd == 0) {
    const u32* wn = (const u32*)(ws + WS_WT) + (size_t)256 * SEQ;
#pragma unroll
    for (int k2 = 0; k2 < 8; ++k2) wx[k2] = wn[kbase + 256 * k2];
  }
  Tw512 tw;
  make_tw512(tw, lane);
  f32x2 t4[2][3];
#pragma unroll
  for (int h = 0; h < 2; ++h)
#pragma unroll
    for (int k = 1; k < 4; ++k) t4[h][k - 1] = twid((float)(((2 * tid + h) * k) & 2047) * (1.0f / 2048.0f));
  {
    f32x2 y[2][4];
    dft4<-1>(q[0].xy, q[1].xy, q[2].xy, q[3].xy, y[0][0], y[0][1], y[0][2], y[0][3]);
    dft4<-1>(q[0].zw, q[1].zw, q[2].zw, q[3].zw, y[1][0], y[1][1], y[1][2], y[1][3]);
#pragma unroll
    for (int k = 0; k < 4; ++k) {
      f32x2 a = y[0][k], b = y[1][k];
      if (k) { a = cmul(a, t4[0][k - 1]); b = cmul(b, t4[1][k - 1]); }
      f32x4 o; o.xy = a; o.zw = b;
      *(f32x4*)(lds + k * PS + 2 * tid) = o;
    }
  }
  SYNC();
  TS(1);
  f32x2* buf = lds + wave * PS;
  f32x2 v[8];
#pragma unroll
  for (int j = 0; j < 8; ++j) v[j] = buf[lane + 64 * j];
  fft512_fwd(v, buf, tw, lane);
  TS(2);
  if (kd != 0) {
#pragma unroll
    for (int k2 = 0; k2 < 8; ++k2) v[k2] = cmul(v[k2], unpk(wv[k2]));
  } else {
    f32x4* ex = (f32x4*)(lds + 4 * PS);
#pragma unroll
    for (int k2 = 0; k2 < 8; ++k2) {
      f32x4 e; e.xy = v[k2]; e.z = U2F(wv[k2]); e.w = U2F(wx[k2]);
      ex[kbase + 256 * k2] = e;
    }
    SYNC();
#pragma unroll
    for (int k2 = 0; k2 < 8; ++k2) {
      const int k = kbase + 256 * k2, kn = (2048 - k) & 2047;
      const f32x4 e = ex[kn];
      const f32x2 c1 = v[k2], c2 = e.xy;
      f32x2 r0, r1;
      r0.x = 0.5f * (c1.x + c2.x); r0.y = 0.5f * (c1.y - c2.y);
      r1.x = 0.5f * (c1.y + c2.y); r1.y = 0.5f * (c2.x - c1.x);
      const u32x4 eu = __builtin_bit_cast(u32x4, e);
      const u32 ez = eu.z, ew = eu.w;
      const f32x2 a1 = unpk(wv[k2]), a2 = unpk(ez), b1 = unpk(wx[k2]), b2 = unpk(ew);
      f32x2 h0, h1;
      h0.x = 0.5f * (a1.x + a2.x); h0.y = 0.5f * (a1.y - a2.y);
      h1.x = 0.5f * (b1.x + b2.x); h1.y = 0.5f * (b1.y - b2.y);
      const f32x2 p0 = cmul(r0, h0), p1 = cmul(r1, h1);
      v[k2].x = p0.x - p1.y;
      v[k2].y = p0.y + p1.x;
    }
  }
  fft512_inv(v, buf, tw, lane);
  TS(3);
  wave_sync();
#pragma unroll
  for (int j = 0; j < 8; ++j) buf[lane + 64 * j] = v[j];
  SYNC();
  {
    f32x2 y[2][4];
#pragma unroll
    for (int k = 0; k < 4; ++k) {
      const f32x4 o = *(const f32x4*)(lds + k * PS + 2 * tid);
      y[0][k] = o.xy; y[1][k] = o.zw;
      if (k) { y[0][k] = cmulc(y[0][k], t4[0][k - 1]); y[1][k] = cmulc(y[1][k], t4[1][k - 1]); }
    }
    f32x2 o[2][4];
    dft4<1>(y[0][0], y[0][1], y[0][2], y[0][3], o[0][0], o[0][1], o[0][2], o[0][3]);
    dft4<1>(y[1][0], y[1][1], y[1][2], y[1][3], o[1][0], o[1][1], o[1][2], o[1][3]);
    const rsrc_t rws = mkrsrc(ws);
#pragma unroll
    for (int n3 = 0; n3 < 4; ++n3) {
      f32x4 w; w.xy = o[0][n3]; w.zw = o[1][n3];
      st16(rws, (u32)WS_YT + (u32)(kd * SEQ * 8 + (tid + 256 * n3) * 16), __builtin_bit_cast(u32x4, w));
    }
  }
  TS(4);
}

NOPK __global__ __launch_bounds__(256, 2) void k_l2(char* __restrict__ ws) {
  extern __shared__ __attribute__((aligned(16))) char smem[];
  if (BIDX >= 256) g2_body(ws, smem, BIDX - 256);
  else k2_body(ws, smem, BIDX);
}

constexpr int PS3 = 648;
NOPK __global__ __launch_bounds__(256) void k_l3(const char* __restrict__ ws, u16* __restrict__ out) {
  __shared__ __attribute__((aligned(16))) f32x2 lds[4 * PS3];
  const int tid = TIDX, lane = tid & 63;
  const int wave = __builtin_amdgcn_readfirstlane(tid >> 6);
  const int s0 = BIDX * 8;
  constexpr int TSK = 2;
  TS(0);
  const f32x2* zt = (const f32x2*)(ws + WS_YT);
  f32x4 z[4];
#pragma unroll
  for (int r = 0; r < 4; ++r) {
    const int i = tid + 256 * r;
    z[r] = *(const f32x4*)(zt + (size_t)(i >> 2) * SEQ + s0 + 2 * (i & 3));
  }
  const int row = s0 + 2 * wave;
  const u16* bb = (const u16*)(ws + WS_B) + (size_t)row * DM;
  const u32x4 b0 = *(const u32x4*)(bb + lane * 8), b1 = *(const u32x4*)(bb + DM + lane * 8);
  const float g0 = ((const float*)(ws + WS_G))[row], g1 = ((const float*)(ws + WS_G))[row + 1];
  Tw512 tw;
  make_tw512(tw, lane);
#pragma unroll
  for (int r = 0; r < 4; ++r) {
    const int i = tid + 256 * r;
    const int kd = i >> 2, p = i & 3;
    const f32x4 q = z[r];
    f32x2 c1, c2;
    if (kd) {
      c1.x = q.x - q.w; c1.y = q.y + q.z;
      c2.x = q.x + q.w; c2.y = q.z - q.y;
    } else {
      c1.x = q.x; c1.y = q.z;
      c2.x = q.y; c2.y = q.w;
    }
    lds[p * PS3 + sslot(kd)] = c1;
    lds[p * PS3 + sslot(kd ? 512 - kd : 256)] = c2;
  }
  SYNC();
  TS(1);
  f32x2* buf = lds + wave * PS3;
  f32x2 v[8];
  {
    const int hi = lane >> 3, lo = lane & 7;
#pragma unroll
    for (int k2 = 0; k2 < 8; ++k2) v[k2] = buf[sslot(hi + 8 * lo + 64 * k2)];
  }
  fft512_inv(v, buf, tw, lane);
  wave_sync();
#pragma unroll
  for (int j = 0; j < 8; ++j) buf[lane + 2 * (lane >> 3) + 80 * j] = v[j];
  wave_sync();
  f32x4 u[4];
#pragma unroll
  for (int j = 0; j < 4; ++j) u[j] = *(const f32x4*)(buf + 10 * lane + 2 * j);
  TS(2);
  const float sc = 1.0f / (2048.0f * 512.0f);
  const float og0 = bfr(1.0f - g0), og1 = bfr(1.0f - g1);
  u32x4 o0, o1;
#pragma unroll
  for (int j = 0; j < 4; ++j) {
    const f32x2 bv0 = unpk(b0[j]), bv1 = unpk(b1[j]);
    o0[j] = pack2(bfr(g0 * bfr(u[j].x * sc)) + bfr(og0 * bv0.x), bfr(g0 * bfr(u[j].z * sc)) + bfr(og0 * bv0.y));
    o1[j] = pack2(bfr(g1 * bfr(u[j].y * sc)) + bfr(og1 * bv1.x), bfr(g1 * bfr(u[j].w * sc)) + bfr(og1 * bv1.y));
  }
  const rsrc_t ro = mkrsrc(out);
  st16(ro, (u32)(row * DM + lane * 8) * 2, o0);
  st16(ro, (u32)((row + 1) * DM + lane * 8) * 2, o1);
  TS(3);
}

extern "C" void kernel_launch(void* const* d_in, const int* in_sizes, int n_in, void* d_out, int out_size,
                              void* d_ws, size_t ws_size, hipStream_t stream) {
  const u16* x = (const u16*)d_in[0];
  const u16* wr = (const u16*)d_in[1];
  const u16* wi = (const u16*)d_in[2];
  const u16* wg = (const u16*)d_in[4];
  const u16* wu = (const u16*)d_in[5];
  const u16* wd = (const u16*)d_in[6];
  const u16* gw = (const u16*)d_in[7];
  char* ws = (char*)d_ws;
  (void)hipFuncSetAttribute((const void*)k_l1, hipFuncAttributeMaxDynamicSharedMemorySize, L1_LDS);
  (void)hipFuncSetAttribute((const void*)k_l2, hipFuncAttributeMaxDynamicSharedMemorySize, L2_LDS);
  k_l1<<<256, 1024, L1_LDS, stream>>>(x, wr, wi, wg, wu, wd, gw, ws);
  k_l2<<<512, 256, L2_LDS, stream>>>(ws);
  k_l3<<<256, 256, 0, stream>>>(ws, (u16*)d_out);
}
```

```cpp
#include <hip/hip_runtime.h>
#include <hip/hip_bf16.h>
#include <stdint.h>

#if defined(__HIP_DEVICE_COMPILE__)
#define NOPK __attribute__((target("no-packed-fp32-ops")))
#else
#define NOPK
#endif
#define DEVI NOPK __device__ __forceinline__
#define TS(i)

typedef __attribute__((ext_vector_type(8))) short bf16x8;
typedef __attribute__((ext_vector_type(4))) short bf16x4;
typedef __attribute__((ext_vector_type(4))) float f32x4;
typedef __attribute__((ext_vector_type(2))) float f32x2;
typedef unsigned short u16;
typedef unsigned int u32;
typedef __attribute__((ext_vector_type(4))) unsigned int u32x4;
typedef __attribute__((ext_vector_type(2))) unsigned int u32x2;
typedef __attribute__((address_space(3))) bf16x4* lds_b4_ptr;
#define TIDX ((int)__builtin_amdgcn_workitem_id_x())
#define BIDX ((int)__builtin_amdgcn_workgroup_id_x())
#define U2F(x) __builtin_bit_cast(float, (u32)(x))
#define SYNC() do { __builtin_amdgcn_fence(__ATOMIC_RELEASE, "workgroup"); __builtin_amdgcn_s_barrier(); \
                    __builtin_amdgcn_fence(__ATOMIC_ACQUIRE, "workgroup"); } while (0)
DEVI float fexp(float x) { return __builtin_amdgcn_exp2f(x * 1.44269504088896340736f); }
DEVI float shfl_xor(float v, int m, int lane) {
  return __builtin_bit_cast(float, __builtin_amdgcn_ds_bpermute((lane ^ m) << 2, __builtin_bit_cast(int, v)));
}

constexpr int SEQ = 2048, DM = 512, DF = 257, FF = 1376;
constexpr int KS32 = FF / 32;

constexpr size_t WS_YT = 0;
constexpr size_t WS_WT = WS_YT + (size_t)DF * SEQ * 8;
constexpr size_t WS_H = WS_WT + (size_t)DF * SEQ * 4;
constexpr size_t WS_WD = WS_H + (size_t)SEQ * FF * 2;
constexpr size_t WS_B = WS_WD + (size_t)FF * DM * 2;
constexpr size_t WS_G = WS_B + (size_t)SEQ * DM * 2;
constexpr size_t WS_SYNC = WS_G + 8192;
constexpr size_t WS_ZT = WS_SYNC + 4096;

DEVI float bf2f(u16 v) { return U2F(((u32)v) << 16); }
DEVI u16 f2bf(float f) {
  __bf16 b = (__bf16)f;
  return __builtin_bit_cast(u16, b);
}
DEVI float bfr(float f) { return bf2f(f2bf(f)); }

DEVI f32x2 cmul(f32x2 a, f32x2 b) {
  f32x2 r;
  r.x = a.x * b.x - a.y * b.y;
  r.y = a.x * b.y + a.y * b.x;
  return r;
}
DEVI f32x2 cmulc(f32x2 a, f32x2 b) {
  f32x2 r;
  r.x = a.x * b.x + a.y * b.y;
  r.y = a.y * b.x - a.x * b.y;
  return r;
}
DEVI f32x2 twid(float frac) {
  f32x2 r;
  r.x = __builtin_amdgcn_cosf(frac);
  r.y = -__builtin_amdgcn_sinf(frac);
  return r;
}

template <int SGN>
DEVI f32x2 mul_i(f32x2 z) {
  f32x2 r;
  if (SGN < 0) { r.x = z.y; r.y = -z.x; } else { r.x = -z.y; r.y = z.x; }
  return r;
}
template <int SGN>
DEVI void dft4(f32x2 b0, f32x2 b1, f32x2 b2, f32x2 b3, f32x2& y0, f32x2& y1, f32x2& y2,
                                     f32x2& y3) {
  f32x2 c0 = b0 + b2, c1 = b0 - b2, c2 = b1 + b3, c3 = mul_i<SGN>(b1 - b3);
  y0 = c0 + c2; y2 = c0 - c2; y1 = c1 + c3; y3 = c1 - c3;
}
template <int SGN>
DEVI void dft8(f32x2 (&v)[8]) {
  const float R = 0.70710678118654752440f;
  f32x2 a0 = v[0] + v[4], a4 = v[0] - v[4];
  f32x2 a1 = v[1] + v[5], a5 = v[1] - v[5];
  f32x2 a2 = v[2] + v[6], a6 = v[2] - v[6];
  f32x2 a3 = v[3] + v[7], a7 = v[3] - v[7];
  f32x2 t5, t7;
  if (SGN < 0) {
    t5.x = (a5.x + a5.y) * R; t5.y = (a5.y - a5.x) * R;
    t7.x = (a7.y - a7.x) * R; t7.y = -(a7.x + a7.y) * R;
  } else {
    t5.x = (a5.x - a5.y) * R; t5.y = (a5.x + a5.y) * R;
    t7.x = -(a7.x + a7.y) * R; t7.y = (a7.x - a7.y) * R;
  }
  f32x2 t6 = mul_i<SGN>(a6);
  dft4<SGN>(a0, a1, a2, a3, v[0], v[2], v[4], v[6]);
  dft4<SGN>(a4, t5, t6, t7, v[1], v[3], v[5], v[7]);
}

DEVI void wave_sync() {
  asm volatile("s_waitcnt lgkmcnt(0)" ::: "memory");
  __builtin_amdgcn_wave_barrier();
}

struct Tw512 {
  f32x2 a[7];
  f32x2 b[7];
};
DEVI void make_tw512(Tw512& t, int lane) {
#pragma unroll
  for (int k = 1; k < 8; ++k) {
    t.a[k - 1] = twid((float)((lane * k) & 511) * (1.0f / 512.0f));
    t.b[k - 1] = twid((float)(((lane & 7) * k) & 63) * (1.0f / 64.0f));
  }

}

DEVI void fft512_fwd(f32x2 (&v)[8], f32x2* buf, const Tw512& tw, int lane) {
  const int hi = lane >> 3, lo = lane & 7;
  dft8<-1>(v);
#pragma unroll
  for (int k = 1; k < 8; ++k) v[k] = cmul(v[k], tw.a[k - 1]);
  wave_sync();
#pragma unroll
  for (int k = 0; k < 8; ++k) buf[72 * k + lane] = v[k];
  wave_sync();
#pragma unroll
  for (int n = 0; n < 8; ++n) v[n] = buf[72 * hi + 8 * n + lo];
  dft8<-1>(v);
#pragma unroll
  for (int k = 1; k < 8; ++k) v[k] = cmul(v[k], tw.b[k - 1]);
  wave_sync();
#pragma unroll
  for (int k = 0; k < 8; ++k) buf[72 * hi + 9 * k + lo] = v[k];
  wave_sync();
#pragma unroll
  for (int n = 0; n < 8; ++n) v[n] = buf[72 * hi + 9 * lo + n];
  dft8<-1>(v);
}
DEVI void fft512_inv(f32x2 (&v)[8], f32x2* buf, const Tw512& tw, int lane) {
  const int hi = lane >> 3, lo = lane & 7;
  dft8<1>(v);
  wave_sync();
#pragma unroll
  for (int n = 0; n < 8; ++n) buf[72 * hi + 9 * lo + n] = v[n];
  wave_sync();
#pragma unroll
  for (int k = 0; k < 8; ++k) v[k] = buf[72 * hi + 9 * k + lo];
#pragma unroll
  for (int k = 1; k < 8; ++k) v[k] = cmulc(v[k], tw.b[k - 1]);
  dft8<1>(v);
  wave_sync();
#pragma unroll
  for (int n = 0; n < 8; ++n) buf[72 * hi + 8 * n + lo] = v[n];
  wave_sync();
#pragma unroll
  for (int k = 0; k < 8; ++k) v[k] = buf[72 * k + lane];
#pragma unroll
  for (int k = 1; k < 8; ++k) v[k] = cmulc(v[k], tw.a[k - 1]);
  dft8<1>(v);
}

DEVI int sslot(int k) { return k + 4 * (k >> 5); }
constexpr int PS = 584;

#define PREFETCH 0
constexpr int G1_NS = 3;
constexpr int G1_X_OFF = G1_NS * 24576;
constexpr int AX_OFF = G1_X_OFF + G1_NS * 16384;
constexpr int AX_W_OFF = AX_OFF + 4 * PS * 8;
constexpr int AX_WD_OFF = AX_W_OFF + 10240;
constexpr int AX_GW_OFF = AX_WD_OFF;
constexpr int L1_CNT_OFF = AX_WD_OFF + 8192;
constexpr int L1_LDS = L1_CNT_OFF + 256;

#define WAIT_V(n) asm volatile("s_waitcnt vmcnt(" #n ")" ::: "memory")
#define WAIT_L(n) asm volatile("s_waitcnt lgkmcnt(" #n ")" ::: "memory")
#define RAW_BAR() asm volatile("s_barrier" ::: "memory")

typedef __amdgpu_buffer_rsrc_t rsrc_t;
typedef __attribute__((ext_vector_type(4))) int i32x4;
DEVI void dma16(const void* gsrc, u32 lds_dst) {
  u32 keep;
  asm volatile("s_mov_b32 %0, m0\n\ts_mov_b32 m0, %2\n\ts_nop 0\n\tglobal_load_lds_dwordx4 %1, off\n\ts_mov_b32 m0, %0"
               : "=&s"(keep) : "v"(gsrc), "s"(lds_dst) : "memory");
}
DEVI void dma16b(i32x4 rs, u32 voff, u32 soff, u32 lds_dst) {
  u32 keep;
  asm volatile("s_mov_b32 %0, m0\n\ts_mov_b32 m0, %4\n\ts_nop 0\n\tbuffer_load_dwordx4 %1, %2, %3 offen lds\n\ts_mov_b32 m0, %0"
               : "=&s"(keep) : "v"(voff), "s"(rs), "s"(soff), "s"(lds_dst) : "memory");
}
DEVI i32x4 mkrs(const void* p, u32 bytes) {
  const unsigned long long a = (unsigned long long)p;
  i32x4 r;
  r.x = __builtin_amdgcn_readfirstlane((int)(u32)a);
  r.y = __builtin_amdgcn_readfirstlane((int)(u32)(a >> 32) & 0xffff);
  r.z = (int)bytes;
  r.w = 0x00020000;
  return r;
}
DEVI rsrc_t mkrsrc(const void* p) { return __builtin_amdgcn_make_buffer_rsrc((void*)p, 0, 0x7fffffff, 0x00020000); }
DEVI void st16(rsrc_t r, u32 off, u32x4 v) { __builtin_amdgcn_raw_buffer_store_b128(v, r, off, 0, 16); }
DEVI u32 pack2(float a, float b) { return (u32)f2bf(a) | ((u32)f2bf(b) << 16); }

typedef __attribute__((address_space(3))) u32* lds_u32_ptr;
DEVI void lds_signal(u32 addr, int lane) {
  unsigned long long keep;
  asm volatile("s_mov_b64 %0, exec\n\ts_mov_b64 exec, 1\n\tds_add_u32 %1, %2\n\ts_mov_b64 exec, %0"
               : "=&s"(keep) : "v"(addr), "v"(1u) : "memory");
}
DEVI void lds_wait1(u32 addr, u32 need) {
  while (true) {
    u32 v;
    asm volatile("ds_read_b32 %0, %1\n\ts_waitcnt lgkmcnt(0)" : "=v"(v) : "v"(addr) : "memory");
    if ((u32)__builtin_amdgcn_readfirstlane(v) >= need) break;
    __builtin_amdgcn_s_sleep(1);
  }
}
DEVI void lds_wait2(u32 addr, u32 need0, u32 need1) {
  while (true) {
    u32x2 v;
    asm volatile("ds_read_b64 %0, %1\n\ts_waitcnt lgkmcnt(0)" : "=v"(v) : "v"(addr) : "memory");
    const u32 a = __builtin_amdgcn_readfirstlane(v.x), b = __builtin_amdgcn_readfirstlane(v.y);
    if (a >= need0 && b >= need1) break;
    __builtin_amdgcn_s_sleep(1);
  }
}

DEVI void aux_body(const u16* __restrict__ x, const u16* __restrict__ wr, const u16* __restrict__ wi,
                   const u16* __restrict__ wd, const u16* __restrict__ gw, char* __restrict__ ws, char* smem, int a,
                   int wave, int lane) {
  constexpr int TSK = 0;
  const u32 lds0 = (u32)(size_t)(__attribute__((address_space(3))) char*)smem;
  const rsrc_t rws = mkrsrc(ws);
  const int s0 = a * 8;
  const int tid = wave * 64 + lane;
  f32x2* aux = (f32x2*)(smem + AX_OFF);
  {
    const int p = wave;
    Tw512 tw;
    make_tw512(tw, lane);
    WAIT_V(0);
    TS(1);
    const u16* xa = (const u16*)(aux + p * PS);
    const u16* gl = (const u16*)(smem + AX_GW_OFF + p * 1024);
    u16 ra[8], rb[8], rg[8];
#pragma unroll
    for (int j = 0; j < 8; ++j) {
      ra[j] = xa[lane + 64 * j];
      rb[j] = xa[DM + lane + 64 * j];
      rg[j] = gl[lane + 64 * j];
    }
    f32x2 v[8];
    float sa = 0.f, sb = 0.f;
#pragma unroll
    for (int j = 0; j < 8; ++j) {
      v[j].x = bf2f(ra[j]);
      v[j].y = bf2f(rb[j]);
      float w = bf2f(rg[j]);
      sa += bfr(v[j].x * w);
      sb += bfr(v[j].y * w);
    }
    {
      WAIT_L(0);
      const char* pr = (const char*)wr + (size_t)a * 4112;
      const char* pi = (const char*)wi + (size_t)a * 4112;
      dma16(pr + p * 1024 + lane * 16, lds0 + AX_W_OFF + p * 1024);
      dma16(pi + p * 1024 + lane * 16, lds0 + AX_W_OFF + 5120 + p * 1024);
      if (p < 2) dma16((p ? pi : pr) + (lane == 0 ? 4096 : 0), lds0 + AX_W_OFF + p * 5120 + 4096);
      const char* pd = (const char*)wd + (size_t)(a < 172 ? a : 0) * 8192;
      dma16(pd + p * 1024 + lane * 16, lds0 + AX_WD_OFF + p * 1024);
      dma16(pd + 4096 + p * 1024 + lane * 16, lds0 + AX_WD_OFF + 4096 + p * 1024);
    }
#pragma unroll
    for (int o = 32; o > 0; o >>= 1) {
      sa += shfl_xor(sa, o, lane);
      sb += shfl_xor(sb, o, lane);
    }
    if (lane < 2) {
      float s = bfr(lane ? sb : sa);
      float gg = bfr(1.0f / (1.0f + fexp(-s)));
      ((float*)(ws + WS_G))[s0 + 2 * p + lane] = gg;
    }
    f32x2* buf = aux + p * PS;
    fft512_fwd(v, buf, tw, lane);
    wave_sync();
    const int hi = lane >> 3, lo = lane & 7;
#pragma unroll
    for (int k2 = 0; k2 < 8; ++k2) buf[sslot(hi + 8 * lo + 64 * k2)] = v[k2];
  }
  WAIT_L(0);
  lds_signal(lds0 + L1_CNT_OFF + 42 * 4, lane);
  WAIT_V(0);
  lds_signal(lds0 + L1_CNT_OFF + 43 * 4, lane);
  TS(2);
  lds_wait1(lds0 + L1_CNT_OFF + 42 * 4, 4);
#pragma unroll
  for (int r = 0; r < 4; ++r) {
    const int i = tid + 256 * r;
    const int kd = i >> 2, p = i & 3;
    const f32x2 z1 = aux[p * PS + sslot(kd)];
    const f32x2 z2 = aux[p * PS + sslot(kd ? 512 - kd : 256)];
    f32x4 o;
    if (kd) {
      o.x = 0.5f * (z1.x + z2.x);
      o.y = 0.5f * (z1.y - z2.y);
      o.z = 0.5f * (z1.y + z2.y);
      o.w = 0.5f * (z2.x - z1.x);
    } else {
      o.x = z1.x; o.y = z2.x; o.z = z1.y; o.w = z2.y;
    }
    st16(rws, (u32)WS_YT + (u32)(kd * SEQ + s0 + 2 * p) * 8, __builtin_bit_cast(u32x4, o));
  }
  TS(3);
  lds_wait1(lds0 + L1_CNT_OFF + 43 * 4, 4);
  {
    const u16* lr = (const u16*)(smem + AX_W_OFF);
    const u16* li = (const u16*)(smem + AX_W_OFF + 5120);
#pragma unroll
    for (int r = 0; r < 3; ++r) {
      const int i = tid + 256 * r;
      if (i < DF * 2) {
        const int kd = i >> 1, h = i & 1;
        u32x4 o;
#pragma unroll
        for (int q = 0; q < 4; ++q) o[q] = (u32)lr[(h * 4 + q) * DF + kd] | ((u32)li[(h * 4 + q) * DF + kd] << 16);
        st16(rws, (u32)WS_WT + (u32)(kd * SEQ + s0 + h * 4) * 4, o);
      }
    }
  }
  if (a < 172) {
    const u16* ld = (const u16*)(smem + AX_WD_OFF);
#pragma unroll
    for (int r = 0; r < 2; ++r) {
      const int n = tid + 256 * r;
      u32x4 o;
#pragma unroll
      for (int q = 0; q < 4; ++q) o[q] = (u32)ld[(2 * q) * DM + n] | ((u32)ld[(2 * q + 1) * DM + n] << 16);
      const int ks = a >> 2, kq = a & 3;
      st16(rws, (u32)WS_WD + (u32)(((ks * 32 + (n >> 4)) * 64 + (n & 15) + 16 * kq) * 16), o);
    }
  }
  TS(4);
}

NOPK __global__ __launch_bounds__(1024, 4) void k_l1(const u16* __restrict__ x, const u16* __restrict__ wr,
                                                const u16* __restrict__ wi, const u16* __restrict__ wg,
                                                const u16* __restrict__ wu, const u16* __restrict__ wd,
                                                const u16* __restrict__ gw, char* __restrict__ ws) {
  extern __shared__ __attribute__((aligned(16))) char smem[];
  const int bid = BIDX;
  const int tid = TIDX, lane = tid & 63;
  const int wave = __builtin_amdgcn_readfirstlane(tid >> 6);
  constexpr int TSK = 0;
  TS(0);
  const u32 lds0 = (u32)(size_t)(__attribute__((address_space(3))) char*)smem;
  const u32 cnt = lds0 + L1_CNT_OFF;
  if (wave == 0) { ((u32*)(smem + L1_CNT_OFF))[lane] = 0; WAIT_L(0); }
  RAW_BAR();
  const int xcd = bid & 7, idx = bid >> 3;
  const int T = xcd * 30 + (idx < 30 ? idx : 0);
  const int fg = T / 80, rem = T - fg * 80;
  const int mt_ = rem / 5, fi = rem - mt_ * 5;
  const int m0 = mt_ * 128, f0 = (fg * 5 + fi) * 96;
  if (wave >= 12) {
    __builtin_amdgcn_s_setprio(2);
    const int w = wave - 12;
    if (bid == 255 && w == 0) ((u32*)(ws + WS_SYNC))[lane * 16] = 0;
    const char* px = (const char*)x + (size_t)(bid * 8 + 2 * w) * 1024;
    dma16(px + lane * 16, lds0 + AX_OFF + w * (PS * 8));
    dma16(px + 1024 + lane * 16, lds0 + AX_OFF + w * (PS * 8) + 1024);
    dma16((const char*)gw + lane * 16, lds0 + AX_GW_OFF + w * 1024);
    aux_body(x, wr, wi, wd, gw, ws, smem, bid, w, lane);
    return;
  }
  if (idx >= 30) return;

  {
    const int q4 = wave >= 8 ? wave - 8 : (wave >> 1);
    const i32x4 rx = mkrs(x, SEQ * DM * 2), rg = mkrs(wg, DM * FF * 2), ru = mkrs(wu, DM * FF * 2);
    u32 vx, vw[3];
    {
      const int row = lane >> 3;
      const int c = (lane & 7) ^ row;
      vx = (u32)((m0 + q4 * 32 + row) * DM + c * 8) * 2;
#pragma unroll
      for (int j = 0; j < 3; ++j) {
        const int q = 64 * j + lane;
        const int k = q / 12, cp = q - k * 12;
        const int c2 = cp ^ (((k >> 3) & 1) << 1);
        vw[j] = (u32)((q4 * 16 + k) * FF + f0 + c2 * 8) * 2;
      }
    }
    auto issue = [&](int s, int lo, int hi) NOPK __attribute__((always_inline)) {
      const int slot = s % G1_NS;
      const u32 wdst = lds0 + slot * 24576 + q4 * 3072, xdst = lds0 + G1_X_OFF + slot * 16384 + q4 * 4096;
      const u32 sw = s * (64 * FF * 2);
#pragma unroll
      for (int n = 0; n < 10; ++n) {
        if (n >= lo && n < hi) {
          if (n < 3) dma16b(rg, vw[n], sw, wdst + n * 1024);
          else if (n < 7) dma16b(rx, vx, s * 128 + (n - 3) * 8192, xdst + (n - 3) * 1024);
          else dma16b(ru, vw[n - 7], sw, wdst + 12288 + (n - 7) * 1024);
        }
      }
    };
    if (wave >= 8) {
      issue(1, 0, 10);
      issue(2, 0, 5);
      WAIT_V(5);
      lds_signal(cnt + 1 * 4, lane);
      issue(2, 5, 10);
#pragma unroll 1
      for (int s = 3; s < 8; ++s) {
        lds_wait1(cnt + (16 + s - 3) * 4, 8);
        issue(s, 0, 3);
        WAIT_V(3);
        lds_signal(cnt + (s - 1) * 4, lane);
        issue(s, 3, 10);
      }
      WAIT_V(0);
      lds_signal(cnt + 7 * 4, lane);
      return;
    }
    if (wave & 1) issue(0, 5, 10); else issue(0, 0, 5);
    WAIT_V(0);
    lds_signal(cnt, lane);
  }

  const int wm = wave >> 1, wf = wave & 1;
  const int l15 = lane & 15, lq = lane >> 4;
  int xoff[2][2];
#pragma unroll
  for (int mt = 0; mt < 2; ++mt)
#pragma unroll
    for (int kk = 0; kk < 2; ++kk) {
      const int row = wm * 32 + mt * 16 + l15;
      xoff[mt][kk] = G1_X_OFF + row * 128 + (((kk * 4 + lq) ^ (row & 7)) << 4);
    }
  const int wq = l15 >> 2, wp = l15 & 3;
  int woff[3];
#pragma unroll
  for (int ft = 0; ft < 3; ++ft)
    woff[ft] = (8 * lq + wq) * 192 + (((2 * (wf * 3 + ft) + (wp >> 1)) ^ (2 * (lq & 1))) << 4) + (wp & 1) * 8;

  f32x4 accg[3][2], accu[3][2];
#pragma unroll
  for (int a = 0; a < 3; ++a)
#pragma unroll
    for (int b = 0; b < 2; ++b) { accg[a][b] = (f32x4)0.f; accu[a][b] = (f32x4)0.f; }

  bf16x8 xa[2], xb[2], wgf[3], wuf[3];
  auto rdx = [&](bf16x8(&xf)[2], int xs, int kk) NOPK __attribute__((always_inline)) {
#pragma unroll
    for (int mt = 0; mt < 2; ++mt) xf[mt] = *(const bf16x8*)(smem + xs + xoff[mt][kk]);
  };
  auto rdw = [&](bf16x8(&wfr)[3], int wsl, int kk, int mat) NOPK __attribute__((always_inline)) {
#pragma unroll
    for (int n = 0; n < 3; ++n) {
      const char* base = smem + (wsl + woff[n]) + kk * 6144 + mat * 12288;
      bf16x4 lo = __builtin_amdgcn_ds_read_tr16_b64_v4i16((lds_b4_ptr)(base));
      bf16x4 hi = __builtin_amdgcn_ds_read_tr16_b64_v4i16((lds_b4_ptr)(base + 768));
      wfr[n] = __builtin_shufflevector(lo, hi, 0, 1, 2, 3, 4, 5, 6, 7);
    }
  };
  auto mma = [&](f32x4(&acc)[3][2], bf16x8(&xf)[2], bf16x8(&wfr)[3]) NOPK __attribute__((always_inline)) {
#pragma unroll
    for (int ft = 0; ft < 3; ++ft)
#pragma unroll
      for (int mt = 0; mt < 2; ++mt)
        acc[ft][mt] = __builtin_amdgcn_mfma_f32_16x16x32_bf16(wfr[ft], xf[mt], acc[ft][mt], 0, 0, 0);
  };
  auto interleave = [&]() NOPK __attribute__((always_inline)) {
#pragma unroll
    for (int i = 0; i < 6; ++i) {
      __builtin_amdgcn_sched_group_barrier(0x008, 1, 0);
      __builtin_amdgcn_sched_group_barrier(0x100, 2, 0);
    }
  };
  const volatile u32* cv = (const volatile u32*)(smem + L1_CNT_OFF);

  TS(1);
  lds_wait1(cnt, 8);
  TS(4);
  rdx(xa, 0, 0);
  rdw(wgf, 0, 0, 0);
#define SB() __builtin_amdgcn_sched_barrier(0)
  int slot = 0;
#pragma unroll 1
  for (int S = 0; S < 8; ++S) {
    const int wsl = slot * 24576, xs = slot * 16384;
    slot = slot == G1_NS - 1 ? 0 : slot + 1;
    const int wsn = slot * 24576, xn = slot * 16384;
    SB(); rdw(wuf, wsl, 0, 1); mma(accg, xa, wgf); interleave();
    SB(); rdw(wgf, wsl, 1, 0); rdx(xb, xs, 1); mma(accu, xa, wuf); interleave();
    SB(); rdw(wuf, wsl, 1, 1); const u32 fv = cv[S + 1]; mma(accg, xb, wgf); interleave();
    SB();
    WAIT_L(0);
    lds_signal(cnt + (16 + S) * 4, lane);
    if (S < 7) {
      if ((u32)__builtin_amdgcn_readfirstlane(fv) < 4) lds_wait1(cnt + (S + 1) * 4, 4);
    }
    SB();
    rdw(wgf, wsn, 0, 0); rdx(xa, xn, 0);
    mma(accu, xb, wuf);
    interleave();
  }
  SB();
  TS(5);
  const rsrc_t rws = mkrsrc(ws);
  const int lane2 = __builtin_amdgcn_mbcnt_hi(~0u, __builtin_amdgcn_mbcnt_lo(~0u, 0u));
  const int e15 = lane2 & 15, eq = lane2 >> 4;
#pragma unroll
  for (int ft = 0; ft < 3; ++ft) {
    u32 pk[2][2];
#pragma unroll
    for (int mt = 0; mt < 2; ++mt) {
      float o[4];
#pragma unroll
      for (int r = 0; r < 4; ++r) {
        const float g = bfr(accg[ft][mt][r]);
        const float sl = bfr(g * __builtin_amdgcn_rcpf(1.0f + fexp(-g)));
        const float u = bfr(accu[ft][mt][r]);
        o[r] = sl * u;
      }
      pk[mt][0] = pack2(o[0], o[1]);
      pk[mt][1] = pack2(o[2], o[3]);
    }
    auto s0_ = __builtin_amdgcn_permlane16_swap(pk[0][0], pk[1][0], false, false);
    auto s1_ = __builtin_amdgcn_permlane16_swap(pk[0][1], pk[1][1], false, false);
    u32x4 ov;
    ov.x = s0_[0]; ov.y = s1_[0]; ov.z = s0_[1]; ov.w = s1_[1];
    const int f = f0 + (wf * 3 + ft) * 16 + 8 * (eq >> 1);
    const int m = m0 + wm * 32 + (eq & 1) * 16 + e15;
    if (f < FF)
      st16(rws, (u32)WS_H + (u32)(((m >> 4) * KS32 + (f >> 5)) * 1024 + ((m & 15) + 16 * ((f & 31) >> 3)) * 16), ov);
  }
  TS(6);
}

constexpr int L2_LDS = 4 * PS * 8 + 32768;
constexpr int G2_DEPTH = 4;

DEVI void g2_body(char* __restrict__ ws, char* smem, int bid) {
  const int tid = TIDX, lane = tid & 63;
  const int wave = __builtin_amdgcn_readfirstlane(tid >> 6);
  constexpr int TSK = 1;
  TS(0);
  const int xcd = bid & 7, idx = bid >> 3;
  const int mt64 = xcd * 4 + (idx >> 3), nt64 = idx & 7;
  const char* hb = ws + WS_H + (size_t)(mt64 * 4) * KS32 * 1024 + lane * 16;
  const char* wb = ws + WS_WD + (size_t)(nt64 * 4) * 1024 + lane * 16;
  f32x4 acc[4][4];
#pragma unroll
  for (int a = 0; a < 4; ++a)
#pragma unroll
    for (int b = 0; b < 4; ++b) acc[a][b] = (f32x4)0.f;
  constexpr int NS = G2_DEPTH + 1;
  bf16x8 hf[NS][4], wf[NS][4];
  auto ld = [&](int slot, int i) NOPK __attribute__((always_inline)) {
    int ks = wave + 4 * i;
    ks = ks > KS32 - 1 ? KS32 - 1 : ks;
#pragma unroll
    for (int j = 0; j < 4; ++j) {
      hf[slot][j] = *(const bf16x8*)(hb + ((size_t)j * KS32 + ks) * 1024);
      wf[slot][j] = *(const bf16x8*)(wb + ((size_t)ks * 32 + j) * 1024);
    }
  };
#pragma unroll
  for (int i = 0; i < G2_DEPTH; ++i) ld(i, i);
  __builtin_amdgcn_sched_barrier(0);
#pragma unroll
  for (int i = 0; i < 11; ++i) {
    if (i + G2_DEPTH < 11) ld((i + G2_DEPTH) % NS, i + G2_DEPTH);
    __builtin_amdgcn_sched_barrier(0);
    if (i < 10 || wave < 3) {
#pragma unroll
      for (int nt = 0; nt < 4; ++nt)
#pragma unroll
        for (int mt = 0; mt < 4; ++mt)
          acc[nt][mt] = __builtin_amdgcn_mfma_f32_16x16x32_bf16(wf[i % NS][nt], hf[i % NS][mt], acc[nt][mt], 0, 0, 0);
    }
    __builtin_amdgcn_sched_barrier(0);
  }
  TS(1);
  f32x4* red = (f32x4*)smem;
#pragma unroll
  for (int nt = 0; nt < 4; ++nt) {
    if (nt != wave) {
      const int src = wave < nt ? wave : wave - 1;
#pragma unroll
      for (int mt = 0; mt < 4; ++mt) red[((nt * 3 + src) * 4 + mt) * 64 + lane] = acc[nt][mt];
    }
  }
  SYNC();
  f32x4 fin[4];
#pragma unroll
  for (int nt = 0; nt < 4; ++nt)
    if (nt == wave) {
#pragma unroll
      for (int mt = 0; mt < 4; ++mt) fin[mt] = acc[nt][mt];
    }
  const rsrc_t rws = mkrsrc(ws);
  const int l15 = lane & 15, lq = lane >> 4;
#pragma unroll
  for (int mt = 0; mt < 4; ++mt)
#pragma unroll
    for (int s = 0; s < 3; ++s) fin[mt] += red[((wave * 3 + s) * 4 + mt) * 64 + lane];
#pragma unroll
  for (int pr = 0; pr < 2; ++pr) {
    auto s0_ = __builtin_amdgcn_permlane16_swap(pack2(fin[2 * pr][0], fin[2 * pr][1]),
                                                pack2(fin[2 * pr + 1][0], fin[2 * pr + 1][1]), false, false);
    auto s1_ = __builtin_amdgcn_permlane16_swap(pack2(fin[2 * pr][2], fin[2 * pr][3]),
                                                pack2(fin[2 * pr + 1][2], fin[2 * pr + 1][3]), false, false);
    u32x4 ov;
    ov.x = s0_[0]; ov.y = s1_[0]; ov.z = s0_[1]; ov.w = s1_[1];
    const int m = mt64 * 64 + (2 * pr + (lq & 1)) * 16 + l15;
    const int n = nt64 * 64 + wave * 16 + 8 * (lq >> 1);
    st16(rws, (u32)WS_B + (u32)(m * DM + n) * 2, ov);
  }
  WAIT_V(0);
  SYNC();
  if (tid == 0) __hip_atomic_fetch_add((u32*)(ws + WS_SYNC) + (8 + mt64) * 16, 1u, __ATOMIC_RELAXED, __HIP_MEMORY_SCOPE_AGENT);
  TS(2);
}

DEVI f32x2 unpk(u32 w) {
  f32x2 r;
  r.x = U2F(w << 16);
  r.y = U2F(w & 0xffff0000u);
  return r;
}

constexpr int PS3 = 648;
DEVI u32 code_prefetch(int tid) {
  const char* pc = (const char*)(__builtin_amdgcn_s_getpc() & ~127ull);
  u32 d;
  asm volatile("global_load_dword %0, %1, off" : "=v"(d) : "v"(pc + (tid < 224 ? tid : 0) * 64) : "memory");
  return d;
}
DEVI void k2_body(char* __restrict__ ws, char* smem, int kd, u16* __restrict__ out) {
  const int tid = TIDX, lane = tid & 63;
  const int wave = __builtin_amdgcn_readfirstlane(tid >> 6);
  constexpr int TSK = 1;
  TS(0);
  __builtin_amdgcn_s_setprio(2);
  const f32x4* col = (const f32x4*)(ws + WS_YT + (size_t)kd * SEQ * 8);
  const u32* wt = (const u32*)(ws + WS_WT) + (size_t)kd * SEQ;
  f32x2* lds = (f32x2*)smem;
  f32x4 q[4];
#pragma unroll
  for (int n3 = 0; n3 < 4; ++n3) q[n3] = col[tid + 256 * n3];
  const int hi = lane >> 3, lo = lane & 7;
  const int kbase = wave + 4 * (hi + 8 * lo);
  u32 wv[8];
#pragma unroll
  for (int k2 = 0; k2 < 8; ++k2) wv[k2] = wt[kbase + 256 * k2];
  u32 wx[8];
  if (kd == 0) {
    const u32* wn = (const u32*)(ws + WS_WT) + (size_t)256 * SEQ;
#pragma unroll
    for (int k2 = 0; k2 < 8; ++k2) wx[k2] = wn[kbase + 256 * k2];
  }
  Tw512 tw;
  make_tw512(tw, lane);
  f32x2 t4[2][3];
#pragma unroll
  for (int h = 0; h < 2; ++h)
#pragma unroll
    for (int k = 1; k < 4; ++k) t4[h][k - 1] = twid((float)(((2 * tid + h) * k) & 2047) * (1.0f / 2048.0f));
  {
    f32x2 y[2][4];
    dft4<-1>(q[0].xy, q[1].xy, q[2].xy, q[3].xy, y[0][0], y[0][1], y[0][2], y[0][3]);
    dft4<-1>(q[0].zw, q[1].zw, q[2].zw, q[3].zw, y[1][0], y[1][1], y[1][2], y[1][3]);
#pragma unroll
    for (int k = 0; k < 4; ++k) {
      f32x2 a = y[0][k], b = y[1][k];
      if (k) { a = cmul(a, t4[0][k - 1]); b = cmul(b, t4[1][k - 1]); }
      f32x4 o; o.xy = a; o.zw = b;
      *(f32x4*)(lds + k * PS + 2 * tid) = o;
    }
  }
  SYNC();
  TS(1);
  f32x2* buf = lds + wave * PS;
  f32x2 v[8];
#pragma unroll
  for (int j = 0; j < 8; ++j) v[j] = buf[lane + 64 * j];
  fft512_fwd(v, buf, tw, lane);
  TS(2);
  if (kd != 0) {
#pragma unroll
    for (int k2 = 0; k2 < 8; ++k2) v[k2] = cmul(v[k2], unpk(wv[k2]));
  } else {
    f32x4* ex = (f32x4*)(lds + 4 * PS);
#pragma unroll
    for (int k2 = 0; k2 < 8; ++k2) {
      f32x4 e; e.xy = v[k2]; e.z = U2F(wv[k2]); e.w = U2F(wx[k2]);
      ex[kbase + 256 * k2] = e;
    }
    SYNC();
#pragma unroll
    for (int k2 = 0; k2 < 8; ++k2) {
      const int k = kbase + 256 * k2, kn = (2048 - k) & 2047;
      const f32x4 e = ex[kn];
      const f32x2 c1 = v[k2], c2 = e.xy;
      f32x2 r0, r1;
      r0.x = 0.5f * (c1.x + c2.x); r0.y = 0.5f * (c1.y - c2.y);
      r1.x = 0.5f * (c1.y + c2.y); r1.y = 0.5f * (c2.x - c1.x);
      const u32x4 eu = __builtin_bit_cast(u32x4, e);
      const u32 ez = eu.z, ew = eu.w;
      const f32x2 a1 = unpk(wv[k2]), a2 = unpk(ez), b1 = unpk(wx[k2]), b2 = unpk(ew);
      f32x2 h0, h1;
      h0.x = 0.5f * (a1.x + a2.x); h0.y = 0.5f * (a1.y - a2.y);
      h1.x = 0.5f * (b1.x + b2.x); h1.y = 0.5f * (b1.y - b2.y);
      const f32x2 p0 = cmul(r0, h0), p1 = cmul(r1, h1);
      v[k2].x = p0.x - p1.y;
      v[k2].y = p0.y + p1.x;
    }
  }
  fft512_inv(v, buf, tw, lane);
  TS(3);
  wave_sync();
#pragma unroll
  for (int j = 0; j < 8; ++j) buf[lane + 64 * j] = v[j];
  SYNC();
  {
    f32x2 y[2][4];
#pragma unroll
    for (int k = 0; k < 4; ++k) {
      const f32x4 o = *(const f32x4*)(lds + k * PS + 2 * tid);
      y[0][k] = o.xy; y[1][k] = o.zw;
      if (k) { y[0][k] = cmulc(y[0][k], t4[0][k - 1]); y[1][k] = cmulc(y[1][k], t4[1][k - 1]); }
    }
    f32x2 o[2][4];
    dft4<1>(y[0][0], y[0][1], y[0][2], y[0][3], o[0][0], o[0][1], o[0][2], o[0][3]);
    dft4<1>(y[1][0], y[1][1], y[1][2], y[1][3], o[1][0], o[1][1], o[1][2], o[1][3]);
    const rsrc_t rw2 = mkrsrc(ws);
#pragma unroll
    for (int n3 = 0; n3 < 4; ++n3) {
      f32x4 w; w.xy = o[0][n3]; w.zw = o[1][n3];
      st16(rw2, (u32)WS_ZT + (u32)(kd * SEQ * 8 + (tid + 256 * n3) * 16), __builtin_bit_cast(u32x4, w));
    }
  }
  WAIT_V(0);
  SYNC();
  u32* sync = (u32*)(ws + WS_SYNC);
  if (tid == 0) {
    if (__hip_atomic_fetch_add(sync + (kd & 7) * 16, 1u, __ATOMIC_RELAXED, __HIP_MEMORY_SCOPE_AGENT) == 31)
      __hip_atomic_fetch_add(sync + 40 * 16, 1u, __ATOMIC_RELAXED, __HIP_MEMORY_SCOPE_AGENT);
  }
  TS(4);

  const int s0 = kd * 8;
  const int row = s0 + 2 * wave;
  const float g0 = ((const float*)(ws + WS_G))[row], g1 = ((const float*)(ws + WS_G))[row + 1];
  if (wave == 0) {
    for (int it = 0; it < (1 << 22); ++it) {
      if (__hip_atomic_load(sync + 40 * 16, __ATOMIC_RELAXED, __HIP_MEMORY_SCOPE_AGENT) >= 8) break;
      __builtin_amdgcn_s_sleep(4);
    }
  }
  SYNC();
  TS(5);
  const rsrc_t rws = mkrsrc(ws);
  u32x4 z[4];
#pragma unroll
  for (int r = 0; r < 4; ++r) {
    const int i = tid + 256 * r;
    z[r] = __builtin_amdgcn_raw_buffer_load_b128(rws, (u32)WS_ZT + (u32)((i >> 2) * SEQ + s0 + 2 * (i & 3)) * 8, 0, 16);
  }
#pragma unroll
  for (int r = 0; r < 4; ++r) {
    const int i = tid + 256 * r;
    const int kk = i >> 2, p = i & 3;
    const f32x4 qq = __builtin_bit_cast(f32x4, z[r]);
    f32x2 c1, c2;
    if (kk) {
      c1.x = qq.x - qq.w; c1.y = qq.y + qq.z;
      c2.x = qq.x + qq.w; c2.y = qq.z - qq.y;
    } else {
      c1.x = qq.x; c1.y = qq.z;
      c2.x = qq.y; c2.y = qq.w;
    }
    lds[p * PS3 + sslot(kk)] = c1;
    lds[p * PS3 + sslot(kk ? 512 - kk : 256)] = c2;
  }
  SYNC();
  TS(6);
  f32x2* buf3 = lds + wave * PS3;
#pragma unroll
  for (int k2 = 0; k2 < 8; ++k2) v[k2] = buf3[sslot(hi + 8 * lo + 64 * k2)];
  fft512_inv(v, buf3, tw, lane);
  wave_sync();
#pragma unroll
  for (int j = 0; j < 8; ++j) buf3[lane + 2 * (lane >> 3) + 80 * j] = v[j];
  wave_sync();
  f32x4 u[4];
#pragma unroll
  for (int j = 0; j < 4; ++j) u[j] = *(const f32x4*)(buf3 + 10 * lane + 2 * j);
  TS(7);
  for (int it = 0; it < (1 << 22); ++it) {
    if (__hip_atomic_load(sync + (8 + (s0 >> 6)) * 16, __ATOMIC_RELAXED, __HIP_MEMORY_SCOPE_AGENT) >= 8) break;
    __builtin_amdgcn_s_sleep(2);
  }
  asm volatile("" ::: "memory");
  TS(8);
  const u32x4 b0 = __builtin_amdgcn_raw_buffer_load_b128(rws, (u32)WS_B + (u32)(row * DM + lane * 8) * 2, 0, 16);
  const u32x4 b1 = __builtin_amdgcn_raw_buffer_load_b128(rws, (u32)WS_B + (u32)((row + 1) * DM + lane * 8) * 2, 0, 16);
  const float sc = 1.0f / (2048.0f * 512.0f);
  const float og0 = bfr(1.0f - g0), og1 = bfr(1.0f - g1);
  u32x4 o0, o1;
#pragma unroll
  for (int j = 0; j < 4; ++j) {
    const f32x2 bv0 = unpk(b0[j]), bv1 = unpk(b1[j]);
    o0[j] = pack2(bfr(g0 * bfr(u[j].x * sc)) + bfr(og0 * bv0.x), bfr(g0 * bfr(u[j].z * sc)) + bfr(og0 * bv0.y));
    o1[j] = pack2(bfr(g1 * bfr(u[j].y * sc)) + bfr(og1 * bv1.x), bfr(g1 * bfr(u[j].w * sc)) + bfr(og1 * bv1.y));
  }
  const rsrc_t ro = mkrsrc(out);
  st16(ro, (u32)(row * DM + lane * 8) * 2, o0);
  st16(ro, (u32)((row + 1) * DM + lane * 8) * 2, o1);
  TS(9);
}

NOPK __global__ __launch_bounds__(256, 2) void k_l2(char* __restrict__ ws, u16* __restrict__ out) {
  extern __shared__ __attribute__((aligned(16))) char smem[];
  if (BIDX >= 504) {
    const u32 d = code_prefetch(TIDX);
    WAIT_V(0);
    asm volatile("" ::"v"(d));
  }
  if (BIDX >= 256) g2_body(ws, smem, BIDX - 256);
  else k2_body(ws, smem, BIDX, out);
}

extern "C" void kernel_launch(void* const* d_in, const int* in_sizes, int n_in, void* d_out, int out_size,
                              void* d_ws, size_t ws_size, hipStream_t stream) {
  const u16* x = (const u16*)d_in[0];
  const u16* wr = (const u16*)d_in[1];
  const u16* wi = (const u16*)d_in[2];
  const u16* wg = (const u16*)d_in[4];
  const u16* wu = (const u16*)d_in[5];
  const u16* wd = (const u16*)d_in[6];
  const u16* gw = (const u16*)d_in[7];
  char* ws = (char*)d_ws;
  (void)hipFuncSetAttribute((const void*)k_l1, hipFuncAttributeMaxDynamicSharedMemorySize, L1_LDS);
  (void)hipFuncSetAttribute((const void*)k_l2, hipFuncAttributeMaxDynamicSharedMemorySize, L2_LDS);
  k_l1<<<256, 1024, L1_LDS, stream>>>(x, wr, wi, wg, wu, wd, gw, ws);
  k_l2<<<512, 256, L2_LDS, stream>>>(ws, (u16*)d_out);
}
```

```cpp
#include <hip/hip_runtime.h>
#include <hip/hip_bf16.h>
#include <stdint.h>

#if defined(__HIP_DEVICE_COMPILE__)
#define NOPK __attribute__((target("no-packed-fp32-ops")))
#else
#define NOPK
#endif
#define DEVI NOPK __device__ __forceinline__
#define TS(i)

typedef __attribute__((ext_vector_type(8))) short bf16x8;
typedef __attribute__((ext_vector_type(4))) short bf16x4;
typedef __attribute__((ext_vector_type(4))) float f32x4;
typedef __attribute__((ext_vector_type(2))) float f32x2;
typedef unsigned short u16;
typedef unsigned int u32;
typedef __attribute__((ext_vector_type(4))) unsigned int u32x4;
typedef __attribute__((ext_vector_type(2))) unsigned int u32x2;
typedef __attribute__((address_space(3))) bf16x4* lds_b4_ptr;
#define TIDX ((int)__builtin_amdgcn_workitem_id_x())
#define BIDX ((int)__builtin_amdgcn_workgroup_id_x())
#define U2F(x) __builtin_bit_cast(float, (u32)(x))
#define SYNC() do { __builtin_amdgcn_fence(__ATOMIC_RELEASE, "workgroup"); __builtin_amdgcn_s_barrier(); \
                    __builtin_amdgcn_fence(__ATOMIC_ACQUIRE, "workgroup"); } while (0)
DEVI float fexp(float x) { return __builtin_amdgcn_exp2f(x * 1.44269504088896340736f); }
DEVI float shfl_xor(float v, int m, int lane) {
  return __builtin_bit_cast(float, __builtin_amdgcn_ds_bpermute((lane ^ m) << 2, __builtin_bit_cast(int, v)));
}

constexpr int SEQ = 2048, DM = 512, DF = 257, FF = 1376;
constexpr int KS32 = FF / 32;

constexpr size_t WS_YT = 0;
constexpr size_t WS_WT = WS_YT + (size_t)DF * SEQ * 8;
constexpr size_t WS_H = WS_WT + (size_t)DF * SEQ * 4;
constexpr size_t WS_WD = WS_H + (size_t)SEQ * FF * 2;
constexpr size_t WS_B = WS_WD + (size_t)FF * DM * 2;
constexpr size_t WS_G = WS_B + (size_t)SEQ * DM * 2;
constexpr size_t WS_SYNC = WS_G + 8192;
constexpr size_t WS_ZT = WS_SYNC + 4096;

DEVI float bf2f(u16 v) { return U2F(((u32)v) << 16); }
DEVI u16 f2bf(float f) {
  __bf16 b = (__bf16)f;
  return __builtin_bit_cast(u16, b);
}
DEVI float bfr(float f) { return bf2f(f2bf(f)); }

DEVI f32x2 cmul(f32x2 a, f32x2 b) {
  f32x2 r;
  r.x = a.x * b.x - a.y * b.y;
  r.y = a.x * b.y + a.y * b.x;
  return r;
}
DEVI f32x2 cmulc(f32x2 a, f32x2 b) {
  f32x2 r;
  r.x = a.x * b.x + a.y * b.y;
  r.y = a.y * b.x - a.x * b.y;
  return r;
}
DEVI f32x2 twid(float frac) {
  f32x2 r;
  r.x = __builtin_amdgcn_cosf(frac);
  r.y = -__builtin_amdgcn_sinf(frac);
  return r;
}

template <int SGN>
DEVI f32x2 mul_i(f32x2 z) {
  f32x2 r;
  if (SGN < 0) { r.x = z.y; r.y = -z.x; } else { r.x = -z.y; r.y = z.x; }
  return r;
}
template <int SGN>
DEVI void dft4(f32x2 b0, f32x2 b1, f32x2 b2, f32x2 b3, f32x2& y0, f32x2& y1, f32x2& y2,
                                     f32x2& y3) {
  f32x2 c0 = b0 + b2, c1 = b0 - b2, c2 = b1 + b3, c3 = mul_i<SGN>(b1 - b3);
  y0 = c0 + c2; y2 = c0 - c2; y1 = c1 + c3; y3 = c1 - c3;
}
template <int SGN>
DEVI void dft8(f32x2 (&v)[8]) {
  const float R = 0.70710678118654752440f;
  f32x2 a0 = v[0] + v[4], a4 = v[0] - v[4];
  f32x2 a1 = v[1] + v[5], a5 = v[1] - v[5];
  f32x2 a2 = v[2] + v[6], a6 = v[2] - v[6];
  f32x2 a3 = v[3] + v[7], a7 = v[3] - v[7];
  f32x2 t5, t7;
  if (SGN < 0) {
    t5.x = (a5.x + a5.y) * R; t5.y = (a5.y - a5.x) * R;
    t7.x = (a7.y - a7.x) * R; t7.y = -(a7.x + a7.y) * R;
  } else {
    t5.x = (a5.x - a5.y) * R; t5.y = (a5.x + a5.y) * R;
    t7.x = -(a7.x + a7.y) * R; t7.y = (a7.x - a7.y) * R;
  }
  f32x2 t6 = mul_i<SGN>(a6);
  dft4<SGN>(a0, a1, a2, a3, v[0], v[2], v[4], v[6]);
  dft4<SGN>(a4, t5, t6, t7, v[1], v[3], v[5], v[7]);
}

DEVI void wave_sync() {
  asm volatile("s_waitcnt lgkmcnt(0)" ::: "memory");
  __builtin_amdgcn_wave_barrier();
}

struct Tw512 {
  f32x2 a[7];
  f32x2 b[7];
};
DEVI void make_tw512(Tw512& t, int lane) {
#pragma unroll
  for (int k = 1; k < 8; ++k) {
    t.a[k - 1] = twid((float)((lane * k) & 511) * (1.0f / 512.0f));
    t.b[k - 1] = twid((float)(((lane & 7) * k) & 63) * (1.0f / 64.0f));
  }

}

DEVI void fft512_fwd(f32x2 (&v)[8], f32x2* buf, const Tw512& tw, int lane) {
  const int hi = lane >> 3, lo = lane & 7;
  dft8<-1>(v);
#pragma unroll
  for (int k = 1; k < 8; ++k) v[k] = cmul(v[k], tw.a[k - 1]);
  wave_sync();
#pragma unroll
  for (int k = 0; k < 8; ++k) buf[72 * k + lane] = v[k];
  wave_sync();
#pragma unroll
  for (int n = 0; n < 8; ++n) v[n] = buf[72 * hi + 8 * n + lo];
  dft8<-1>(v);
#pragma unroll
  for (int k = 1; k < 8; ++k) v[k] = cmul(v[k], tw.b[k - 1]);
  wave_sync();
#pragma unroll
  for (int k = 0; k < 8; ++k) buf[72 * hi + 9 * k + lo] = v[k];
  wave_sync();
#pragma unroll
  for (int n = 0; n < 8; ++n) v[n] = buf[72 * hi + 9 * lo + n];
  dft8<-1>(v);
}
DEVI void fft512_inv(f32x2 (&v)[8], f32x2* buf, const Tw512& tw, int lane) {
  const int hi = lane >> 3, lo = lane & 7;
  dft8<1>(v);
  wave_sync();
#pragma unroll
  for (int n = 0; n < 8; ++n) buf[72 * hi + 9 * lo + n] = v[n];
  wave_sync();
#pragma unroll
  for (int k = 0; k < 8; ++k) v[k] = buf[72 * hi + 9 * k + lo];
#pragma unroll
  for (int k = 1; k < 8; ++k) v[k] = cmulc(v[k], tw.b[k - 1]);
  dft8<1>(v);
  wave_sync();
#pragma unroll
  for (int n = 0; n < 8; ++n) buf[72 * hi + 8 * n + lo] = v[n];
  wave_sync();
#pragma unroll
  for (int k = 0; k < 8; ++k) v[k] = buf[72 * k + lane];
#pragma unroll
  for (int k = 1; k < 8; ++k) v[k] = cmulc(v[k], tw.a[k - 1]);
  dft8<1>(v);
}

DEVI int sslot(int k) { return k + 4 * (k >> 5); }
constexpr int PS = 584;

#define PREFETCH 0
#define AUX_GATE 7
constexpr int G1_NS = 3;
constexpr int G1_X_OFF = G1_NS * 24576;
constexpr int AX_OFF = G1_X_OFF + G1_NS * 16384;
constexpr int AX_W_OFF = AX_OFF + 4 * PS * 8;
constexpr int AX_WD_OFF = AX_W_OFF + 10240;
constexpr int AX_GW_OFF = AX_WD_OFF;
constexpr int L1_CNT_OFF = AX_WD_OFF + 8192;
constexpr int L1_LDS = L1_CNT_OFF + 256;

#define WAIT_V(n) asm volatile("s_waitcnt vmcnt(" #n ")" ::: "memory")
#define WAIT_L(n) asm volatile("s_waitcnt lgkmcnt(" #n ")" ::: "memory")
#define RAW_BAR() asm volatile("s_barrier" ::: "memory")

typedef __amdgpu_buffer_rsrc_t rsrc_t;
typedef __attribute__((ext_vector_type(4))) int i32x4;
DEVI void dma16(const void* gsrc, u32 lds_dst) {
  u32 keep;
  asm volatile("s_mov_b32 %0, m0\n\ts_mov_b32 m0, %2\n\ts_nop 0\n\tglobal_load_lds_dwordx4 %1, off\n\ts_mov_b32 m0, %0"
               : "=&s"(keep) : "v"(gsrc), "s"(lds_dst) : "memory");
}
DEVI void dma16b(i32x4 rs, u32 voff, u32 soff, u32 lds_dst) {
  u32 keep;
  asm volatile("s_mov_b32 %0, m0\n\ts_mov_b32 m0, %4\n\ts_nop 0\n\tbuffer_load_dwordx4 %1, %2, %3 offen lds\n\ts_mov_b32 m0, %0"
               : "=&s"(keep) : "v"(voff), "s"(rs), "s"(soff), "s"(lds_dst) : "memory");
}
DEVI i32x4 mkrs(const void* p, u32 bytes) {
  const unsigned long long a = (unsigned long long)p;
  i32x4 r;
  r.x = __builtin_amdgcn_readfirstlane((int)(u32)a);
  r.y = __builtin_amdgcn_readfirstlane((int)(u32)(a >> 32) & 0xffff);
  r.z = (int)bytes;
  r.w = 0x00020000;
  return r;
}
DEVI rsrc_t mkrsrc(const void* p) { return __builtin_amdgcn_make_buffer_rsrc((void*)p, 0, 0x7fffffff, 0x00020000); }
DEVI void st16(rsrc_t r, u32 off, u32x4 v) { __builtin_amdgcn_raw_buffer_store_b128(v, r, off, 0, 16); }
DEVI u32 pack2(float a, float b) { return (u32)f2bf(a) | ((u32)f2bf(b) << 16); }

typedef __attribute__((address_space(3))) u32* lds_u32_ptr;
DEVI void lds_signal(u32 addr, int lane) {
  unsigned long long keep;
  asm volatile("s_mov_b64 %0, exec\n\ts_mov_b64 exec, 1\n\tds_add_u32 %1, %2\n\ts_mov_b64 exec, %0"
               : "=&s"(keep) : "v"(addr), "v"(1u) : "memory");
}
DEVI void lds_wait1(u32 addr, u32 need) {
  while (true) {
    u32 v;
    asm volatile("ds_read_b32 %0, %1\n\ts_waitcnt lgkmcnt(0)" : "=v"(v) : "v"(addr) : "memory");
    if ((u32)__builtin_amdgcn_readfirstlane(v) >= need) break;
    __builtin_amdgcn_s_sleep(1);
  }
}
DEVI void lds_wait2(u32 addr, u32 need0, u32 need1) {
  while (true) {
    u32x2 v;
    asm volatile("ds_read_b64 %0, %1\n\ts_waitcnt lgkmcnt(0)" : "=v"(v) : "v"(addr) : "memory");
    const u32 a = __builtin_amdgcn_readfirstlane(v.x), b = __builtin_amdgcn_readfirstlane(v.y);
    if (a >= need0 && b >= need1) break;
    __builtin_amdgcn_s_sleep(1);
  }
}

DEVI void aux_body(const u16* __restrict__ x, const u16* __restrict__ wr, const u16* __restrict__ wi,
                   const u16* __restrict__ wd, const u16* __restrict__ gw, char* __restrict__ ws, char* smem, int a,
                   int wave, int lane, bool gated) {
  constexpr int TSK = 0;
  const u32 lds0 = (u32)(size_t)(__attribute__((address_space(3))) char*)smem;
  const rsrc_t rws = mkrsrc(ws);
  const int s0 = a * 8;
  const int tid = wave * 64 + lane;
  f32x2* aux = (f32x2*)(smem + AX_OFF);
  {
    const int p = wave;
    Tw512 tw;
    make_tw512(tw, lane);
    WAIT_V(0);
    TS(1);
    const u16* xa = (const u16*)(aux + p * PS);
    const u16* gl = (const u16*)(smem + AX_GW_OFF + p * 1024);
    u16 ra[8], rb[8], rg[8];
#pragma unroll
    for (int j = 0; j < 8; ++j) {
      ra[j] = xa[lane + 64 * j];
      rb[j] = xa[DM + lane + 64 * j];
      rg[j] = gl[lane + 64 * j];
    }
    f32x2 v[8];
    float sa = 0.f, sb = 0.f;
#pragma unroll
    for (int j = 0; j < 8; ++j) {
      v[j].x = bf2f(ra[j]);
      v[j].y = bf2f(rb[j]);
      float w = bf2f(rg[j]);
      sa += bfr(v[j].x * w);
      sb += bfr(v[j].y * w);
    }
#pragma unroll
    for (int o = 32; o > 0; o >>= 1) {
      sa += shfl_xor(sa, o, lane);
      sb += shfl_xor(sb, o, lane);
    }
    if (lane < 2) {
      float s = bfr(lane ? sb : sa);
      float gg = bfr(1.0f / (1.0f + fexp(-s)));
      ((float*)(ws + WS_G))[s0 + 2 * p + lane] = gg;
    }
    f32x2* buf = aux + p * PS;
    fft512_fwd(v, buf, tw, lane);
    wave_sync();
    const int hi = lane >> 3, lo = lane & 7;
#pragma unroll
    for (int k2 = 0; k2 < 8; ++k2) buf[sslot(hi + 8 * lo + 64 * k2)] = v[k2];
  }
  WAIT_L(0);
  lds_signal(lds0 + L1_CNT_OFF + 42 * 4, lane);
  TS(2);
  if (gated) lds_wait1(lds0 + L1_CNT_OFF + 44 * 4, 4);
  {
    const int p = wave;
    const char* pr = (const char*)wr + (size_t)a * 4112;
    const char* pi = (const char*)wi + (size_t)a * 4112;
    dma16(pr + p * 1024 + lane * 16, lds0 + AX_W_OFF + p * 1024);
    dma16(pi + p * 1024 + lane * 16, lds0 + AX_W_OFF + 5120 + p * 1024);
    if (p < 2) dma16((p ? pi : pr) + (lane == 0 ? 4096 : 0), lds0 + AX_W_OFF + p * 5120 + 4096);
    const char* pd = (const char*)wd + (size_t)(a < 172 ? a : 0) * 8192;
    dma16(pd + p * 1024 + lane * 16, lds0 + AX_WD_OFF + p * 1024);
    dma16(pd + 4096 + p * 1024 + lane * 16, lds0 + AX_WD_OFF + 4096 + p * 1024);
  }
  lds_wait1(lds0 + L1_CNT_OFF + 42 * 4, 4);
#pragma unroll
  for (int r = 0; r < 4; ++r) {
    const int i = tid + 256 * r;
    const int kd = i >> 2, p = i & 3;
    const f32x2 z1 = aux[p * PS + sslot(kd)];
    const f32x2 z2 = aux[p * PS + sslot(kd ? 512 - kd : 256)];
    f32x4 o;
    if (kd) {
      o.x = 0.5f * (z1.x + z2.x);
      o.y = 0.5f * (z1.y - z2.y);
      o.z = 0.5f * (z1.y + z2.y);
      o.w = 0.5f * (z2.x - z1.x);
    } else {
      o.x = z1.x; o.y = z2.x; o.z = z1.y; o.w = z2.y;
    }
    st16(rws, (u32)WS_YT + (u32)(kd * SEQ + s0 + 2 * p) * 8, __builtin_bit_cast(u32x4, o));
  }
  TS(3);
  WAIT_V(4);
  lds_signal(lds0 + L1_CNT_OFF + 43 * 4, lane);
  lds_wait1(lds0 + L1_CNT_OFF + 43 * 4, 4);
  {
    const u16* lr = (const u16*)(smem + AX_W_OFF);
    const u16* li = (const u16*)(smem + AX_W_OFF + 5120);
#pragma unroll
    for (int r = 0; r < 3; ++r) {
      const int i = tid + 256 * r;
      if (i < DF * 2) {
        const int kd = i >> 1, h = i & 1;
        u32x4 o;
#pragma unroll
        for (int q = 0; q < 4; ++q) o[q] = (u32)lr[(h * 4 + q) * DF + kd] | ((u32)li[(h * 4 + q) * DF + kd] << 16);
        st16(rws, (u32)WS_WT + (u32)(kd * SEQ + s0 + h * 4) * 4, o);
      }
    }
  }
  if (a < 172) {
    const u16* ld = (const u16*)(smem + AX_WD_OFF);
#pragma unroll
    for (int r = 0; r < 2; ++r) {
      const int n = tid + 256 * r;
      u32x4 o;
#pragma unroll
      for (int q = 0; q < 4; ++q) o[q] = (u32)ld[(2 * q) * DM + n] | ((u32)ld[(2 * q + 1) * DM + n] << 16);
      const int ks = a >> 2, kq = a & 3;
      st16(rws, (u32)WS_WD + (u32)(((ks * 32 + (n >> 4)) * 64 + (n & 15) + 16 * kq) * 16), o);
    }
  }
  TS(4);
}

NOPK __global__ __launch_bounds__(1024, 4) void k_l1(const u16* __restrict__ x, const u16* __restrict__ wr,
                                                const u16* __restrict__ wi, const u16* __restrict__ wg,
                                                const u16* __restrict__ wu, const u16* __restrict__ wd,
                                                const u16* __restrict__ gw, char* __restrict__ ws) {
  extern __shared__ __attribute__((aligned(16))) char smem[];
  const int bid = BIDX;
  const int tid = TIDX, lane = tid & 63;
  const int wave = __builtin_amdgcn_readfirstlane(tid >> 6);
  constexpr int TSK = 0;
  TS(0);
  const u32 lds0 = (u32)(size_t)(__attribute__((address_space(3))) char*)smem;
  const u32 cnt = lds0 + L1_CNT_OFF;
  if (wave == 0) { ((u32*)(smem + L1_CNT_OFF))[lane] = 0; WAIT_L(0); }
  RAW_BAR();
  const int xcd = bid & 7, idx = bid >> 3;
  const int T = xcd * 30 + (idx < 30 ? idx : 0);
  const int fg = T / 80, rem = T - fg * 80;
  const int mt_ = rem / 5, fi = rem - mt_ * 5;
  const int m0 = mt_ * 128, f0 = (fg * 5 + fi) * 96;
  if (wave >= 12) {
    __builtin_amdgcn_s_setprio(2);
    const int w = wave - 12;
    if (bid == 255 && w == 0) ((u32*)(ws + WS_SYNC))[lane * 16] = 0;
    const char* px = (const char*)x + (size_t)(bid * 8 + 2 * w) * 1024;
    dma16(px + lane * 16, lds0 + AX_OFF + w * (PS * 8));
    dma16(px + 1024 + lane * 16, lds0 + AX_OFF + w * (PS * 8) + 1024);
    dma16((const char*)gw + lane * 16, lds0 + AX_GW_OFF + w * 1024);
    aux_body(x, wr, wi, wd, gw, ws, smem, bid, w, lane, idx < 30);
    return;
  }
  if (idx >= 30) return;

  {
    const int q4 = wave >= 8 ? wave - 8 : (wave >> 1);
    const i32x4 rx = mkrs(x, SEQ * DM * 2), rg = mkrs(wg, DM * FF * 2), ru = mkrs(wu, DM * FF * 2);
    u32 vx, vw[3];
    {
      const int row = lane >> 3;
      const int c = (lane & 7) ^ row;
      vx = (u32)((m0 + q4 * 32 + row) * DM + c * 8) * 2;
#pragma unroll
      for (int j = 0; j < 3; ++j) {
        const int q = 64 * j + lane;
        const int k = q / 12, cp = q - k * 12;
        const int c2 = cp ^ (((k >> 3) & 1) << 1);
        vw[j] = (u32)((q4 * 16 + k) * FF + f0 + c2 * 8) * 2;
      }
    }
    auto issue = [&](int s, int lo, int hi) NOPK __attribute__((always_inline)) {
      const int slot = s % G1_NS;
      const u32 wdst = lds0 + slot * 24576 + q4 * 3072, xdst = lds0 + G1_X_OFF + slot * 16384 + q4 * 4096;
      const u32 sw = s * (64 * FF * 2);
#pragma unroll
      for (int n = 0; n < 10; ++n) {
        if (n >= lo && n < hi) {
          if (n < 3) dma16b(rg, vw[n], sw, wdst + n * 1024);
          else if (n < 7) dma16b(rx, vx, s * 128 + (n - 3) * 8192, xdst + (n - 3) * 1024);
          else dma16b(ru, vw[n - 7], sw, wdst + 12288 + (n - 7) * 1024);
        }
      }
    };
    if (wave >= 8) {
      issue(1, 0, 10);
      issue(2, 0, 5);
      WAIT_V(5);
      lds_signal(cnt + 1 * 4, lane);
      issue(2, 5, 10);
#pragma unroll 1
      for (int s = 3; s < 8; ++s) {
        lds_wait1(cnt + (16 + s - 3) * 4, 8);
        issue(s, 0, 3);
        WAIT_V(3);
        lds_signal(cnt + (s - 1) * 4, lane);
        issue(s, 3, 10);
        if (s == AUX_GATE) lds_signal(cnt + 44 * 4, lane);
      }
      WAIT_V(0);
      lds_signal(cnt + 7 * 4, lane);
      return;
    }
    if (wave & 1) issue(0, 5, 10); else issue(0, 0, 5);
    WAIT_V(0);
    lds_signal(cnt, lane);
  }

  const int wm = wave >> 1, wf = wave & 1;
  const int l15 = lane & 15, lq = lane >> 4;
  int xoff[2][2];
#pragma unroll
  for (int mt = 0; mt < 2; ++mt)
#pragma unroll
    for (int kk = 0; kk < 2; ++kk) {
      const int row = wm * 32 + mt * 16 + l15;
      xoff[mt][kk] = G1_X_OFF + row * 128 + (((kk * 4 + lq) ^ (row & 7)) << 4);
    }
  const int wq = l15 >> 2, wp = l15 & 3;
  int woff[3];
#pragma unroll
  for (int ft = 0; ft < 3; ++ft)
    woff[ft] = (8 * lq + wq) * 192 + (((2 * (wf * 3 + ft) + (wp >> 1)) ^ (2 * (lq & 1))) << 4) + (wp & 1) * 8;

  f32x4 accg[3][2], accu[3][2];
#pragma unroll
  for (int a = 0; a < 3; ++a)
#pragma unroll
    for (int b = 0; b < 2; ++b) { accg[a][b] = (f32x4)0.f; accu[a][b] = (f32x4)0.f; }

  bf16x8 xa[2], xb[2], wgf[3], wuf[3];
  auto rdx = [&](bf16x8(&xf)[2], int xs, int kk) NOPK __attribute__((always_inline)) {
#pragma unroll
    for (int mt = 0; mt < 2; ++mt) xf[mt] = *(const bf16x8*)(smem + xs + xoff[mt][kk]);
  };
  auto rdw = [&](bf16x8(&wfr)[3], int wsl, int kk, int mat) NOPK __attribute__((always_inline)) {
#pragma unroll
    for (int n = 0; n < 3; ++n) {
      const char* base = smem + (wsl + woff[n]) + kk * 6144 + mat * 12288;
      bf16x4 lo = __builtin_amdgcn_ds_read_tr16_b64_v4i16((lds_b4_ptr)(base));
      bf16x4 hi = __builtin_amdgcn_ds_read_tr16_b64_v4i16((lds_b4_ptr)(base + 768));
      wfr[n] = __builtin_shufflevector(lo, hi, 0, 1, 2, 3, 4, 5, 6, 7);
    }
  };
  auto mma = [&](f32x4(&acc)[3][2], bf16x8(&xf)[2], bf16x8(&wfr)[3]) NOPK __attribute__((always_inline)) {
#pragma unroll
    for (int ft = 0; ft < 3; ++ft)
#pragma unroll
      for (int mt = 0; mt < 2; ++mt)
        acc[ft][mt] = __builtin_amdgcn_mfma_f32_16x16x32_bf16(wfr[ft], xf[mt], acc[ft][mt], 0, 0, 0);
  };
  auto interleave = [&]() NOPK __attribute__((always_inline)) {
#pragma unroll
    for (int i = 0; i < 6; ++i) {
      __builtin_amdgcn_sched_group_barrier(0x008, 1, 0);
      __builtin_amdgcn_sched_group_barrier(0x100, 2, 0);
    }
  };
  const volatile u32* cv = (const volatile u32*)(smem + L1_CNT_OFF);

  TS(1);
  lds_wait1(cnt, 8);
  TS(4);
  rdx(xa, 0, 0);
  rdw(wgf, 0, 0, 0);
#define SB() __builtin_amdgcn_sched_barrier(0)
  int slot = 0;
#pragma unroll 1
  for (int S = 0; S < 8; ++S) {
    const int wsl = slot * 24576, xs = slot * 16384;
    slot = slot == G1_NS - 1 ? 0 : slot + 1;
    const int wsn = slot * 24576, xn = slot * 16384;
    SB(); rdw(wuf, wsl, 0, 1); mma(accg, xa, wgf); interleave();
    SB(); rdw(wgf, wsl, 1, 0); rdx(xb, xs, 1); mma(accu, xa, wuf); interleave();
    SB(); rdw(wuf, wsl, 1, 1); const u32 fv = cv[S + 1]; mma(accg, xb, wgf); interleave();
    SB();
    WAIT_L(0);
    lds_signal(cnt + (16 + S) * 4, lane);
    if (S < 7) {
      if ((u32)__builtin_amdgcn_readfirstlane(fv) < 4) lds_wait1(cnt + (S + 1) * 4, 4);
    }
    SB();
    rdw(wgf, wsn, 0, 0); rdx(xa, xn, 0);
    mma(accu, xb, wuf);
    interleave();
  }
  SB();
  TS(5);
  const rsrc_t rws = mkrsrc(ws);
  const int lane2 = __builtin_amdgcn_mbcnt_hi(~0u, __builtin_amdgcn_mbcnt_lo(~0u, 0u));
  const int e15 = lane2 & 15, eq = lane2 >> 4;
#pragma unroll
  for (int ft = 0; ft < 3; ++ft) {
    u32 pk[2][2];
#pragma unroll
    for (int mt = 0; mt < 2; ++mt) {
      float o[4];
#pragma unroll
      for (int r = 0; r < 4; ++r) {
        const float g = bfr(accg[ft][mt][r]);
        const float sl = bfr(g * __builtin_amdgcn_rcpf(1.0f + fexp(-g)));
        const float u = bfr(accu[ft][mt][r]);
        o[r] = sl * u;
      }
      pk[mt][0] = pack2(o[0], o[1]);
      pk[mt][1] = pack2(o[2], o[3]);
    }
    auto s0_ = __builtin_amdgcn_permlane16_swap(pk[0][0], pk[1][0], false, false);
    auto s1_ = __builtin_amdgcn_permlane16_swap(pk[0][1], pk[1][1], false, false);
    u32x4 ov;
    ov.x = s0_[0]; ov.y = s1_[0]; ov.z = s0_[1]; ov.w = s1_[1];
    const int f = f0 + (wf * 3 + ft) * 16 + 8 * (eq >> 1);
    const int m = m0 + wm * 32 + (eq & 1) * 16 + e15;
    if (f < FF)
      st16(rws, (u32)WS_H + (u32)(((m >> 4) * KS32 + (f >> 5)) * 1024 + ((m & 15) + 16 * ((f & 31) >> 3)) * 16), ov);
  }
  TS(6);
}

constexpr int L2_LDS = 4 * PS * 8 + 32768;
constexpr int G2_DEPTH = 4;

DEVI void g2_body(char* __restrict__ ws, char* smem, int bid) {
  const int tid = TIDX, lane = tid & 63;
  const int wave = __builtin_amdgcn_readfirstlane(tid >> 6);
  constexpr int TSK = 1;
  TS(0);
  const int xcd = bid & 7, idx = bid >> 3;
  const int mt64 = xcd * 4 + (idx >> 3), nt64 = idx & 7;
  const char* hb = ws + WS_H + (size_t)(mt64 * 4) * KS32 * 1024 + lane * 16;
  const char* wb = ws + WS_WD + (size_t)(nt64 * 4) * 1024 + lane * 16;
  f32x4 acc[4][4];
#pragma unroll
  for (int a = 0; a < 4; ++a)
#pragma unroll
    for (int b = 0; b < 4; ++b) acc[a][b] = (f32x4)0.f;
  constexpr int NS = G2_DEPTH + 1;
  bf16x8 hf[NS][4], wf[NS][4];
  auto ld = [&](int slot, int i) NOPK __attribute__((always_inline)) {
    int ks = wave + 4 * i;
    ks = ks > KS32 - 1 ? KS32 - 1 : ks;
#pragma unroll
    for (int j = 0; j < 4; ++j) {
      hf[slot][j] = *(const bf16x8*)(hb + ((size_t)j * KS32 + ks) * 1024);
      wf[slot][j] = *(const bf16x8*)(wb + ((size_t)ks * 32 + j) * 1024);
    }
  };
#pragma unroll
  for (int i = 0; i < G2_DEPTH; ++i) ld(i, i);
  __builtin_amdgcn_sched_barrier(0);
#pragma unroll
  for (int i = 0; i < 11; ++i) {
    if (i + G2_DEPTH < 11) ld((i + G2_DEPTH) % NS, i + G2_DEPTH);
    __builtin_amdgcn_sched_barrier(0);
    if (i < 10 || wave < 3) {
#pragma unroll
      for (int nt = 0; nt < 4; ++nt)
#pragma unroll
        for (int mt = 0; mt < 4; ++mt)
          acc[nt][mt] = __builtin_amdgcn_mfma_f32_16x16x32_bf16(wf[i % NS][nt], hf[i % NS][mt], acc[nt][mt], 0, 0, 0);
    }
    __builtin_amdgcn_sched_barrier(0);
  }
  TS(1);
  f32x4* red = (f32x4*)smem;
#pragma unroll
  for (int nt = 0; nt < 4; ++nt) {
    if (nt != wave) {
      const int src = wave < nt ? wave : wave - 1;
#pragma unroll
      for (int mt = 0; mt < 4; ++mt) red[((nt * 3 + src) * 4 + mt) * 64 + lane] = acc[nt][mt];
    }
  }
  SYNC();
  f32x4 fin[4];
#pragma unroll
  for (int nt = 0; nt < 4; ++nt)
    if (nt == wave) {
#pragma unroll
      for (int mt = 0; mt < 4; ++mt) fin[mt] = acc[nt][mt];
    }
  const rsrc_t rws = mkrsrc(ws);
  const int l15 = lane & 15, lq = lane >> 4;
#pragma unroll
  for (int mt = 0; mt < 4; ++mt)
#pragma unroll
    for (int s = 0; s < 3; ++s) fin[mt] += red[((wave * 3 + s) * 4 + mt) * 64 + lane];
#pragma unroll
  for (int pr = 0; pr < 2; ++pr) {
    auto s0_ = __builtin_amdgcn_permlane16_swap(pack2(fin[2 * pr][0], fin[2 * pr][1]),
                                                pack2(fin[2 * pr + 1][0], fin[2 * pr + 1][1]), false, false);
    auto s1_ = __builtin_amdgcn_permlane16_swap(pack2(fin[2 * pr][2], fin[2 * pr][3]),
                                                pack2(fin[2 * pr + 1][2], fin[2 * pr + 1][3]), false, false);
    u32x4 ov;
    ov.x = s0_[0]; ov.y = s1_[0]; ov.z = s0_[1]; ov.w = s1_[1];
    const int m = mt64 * 64 + (2 * pr + (lq & 1)) * 16 + l15;
    const int n = nt64 * 64 + wave * 16 + 8 * (lq >> 1);
    st16(rws, (u32)WS_B + (u32)(m * DM + n) * 2, ov);
  }
  WAIT_V(0);
  SYNC();
  if (tid == 0) __hip_atomic_fetch_add((u32*)(ws + WS_SYNC) + (8 + mt64) * 16, 1u, __ATOMIC_RELAXED, __HIP_MEMORY_SCOPE_AGENT);
  TS(2);
}

DEVI f32x2 unpk(u32 w) {
  f32x2 r;
  r.x = U2F(w << 16);
  r.y = U2F(w & 0xffff0000u);
  return r;
}

constexpr int PS3 = 648;
DEVI u32 code_prefetch(int tid) {
  const char* pc = (const char*)(__builtin_amdgcn_s_getpc() & ~127ull);
  u32 d;
  asm volatile("global_load_dword %0, %1, off" : "=v"(d) : "v"(pc + (tid < 224 ? tid : 0) * 64) : "memory");
  return d;
}
DEVI void k2_body(char* __restrict__ ws, char* smem, int kd, u16* __restrict__ out) {
  const int tid = TIDX, lane = tid & 63;
  const int wave = __builtin_amdgcn_readfirstlane(tid >> 6);
  constexpr int TSK = 1;
  TS(0);
  const f32x4* col = (const f32x4*)(ws + WS_YT + (size_t)kd * SEQ * 8);
  const u32* wt = (const u32*)(ws + WS_WT) + (size_t)kd * SEQ;
  f32x2* lds = (f32x2*)smem;
  f32x4 q[4];
#pragma unroll
  for (int n3 = 0; n3 < 4; ++n3) q[n3] = col[tid + 256 * n3];
  const int hi = lane >> 3, lo = lane & 7;
  const int kbase = wave + 4 * (hi + 8 * lo);
  u32 wv[8];
#pragma unroll
  for (int k2 = 0; k2 < 8; ++k2) wv[k2] = wt[kbase + 256 * k2];
  u32 wx[8];
  if (kd == 0) {
    const u32* wn = (const u32*)(ws + WS_WT) + (size_t)256 * SEQ;
#pragma unroll
    for (int k2 = 0; k2 < 8; ++k2) wx[k2] = wn[kbase + 256 * k2];
  }
  Tw512 tw;
  make_tw512(tw, lane);
  f32x2 t4[2][3];
#pragma unroll
  for (int h = 0; h < 2; ++h)
#pragma unroll
    for (int k = 1; k < 4; ++k) t4[h][k - 1] = twid((float)(((2 * tid + h) * k) & 2047) * (1.0f / 2048.0f));
  {
    f32x2 y[2][4];
    dft4<-1>(q[0].xy, q[1].xy, q[2].xy, q[3].xy, y[0][0], y[0][1], y[0][2], y[0][3]);
    dft4<-1>(q[0].zw, q[1].zw, q[2].zw, q[3].zw, y[1][0], y[1][1], y[1][2], y[1][3]);
#pragma unroll
    for (int k = 0; k < 4; ++k) {
      f32x2 a = y[0][k], b = y[1][k];
      if (k) { a = cmul(a, t4[0][k - 1]); b = cmul(b, t4[1][k - 1]); }
      f32x4 o; o.xy = a; o.zw = b;
      *(f32x4*)(lds + k * PS + 2 * tid) = o;
    }
  }
  SYNC();
  TS(1);
  f32x2* buf = lds + wave * PS;
  f32x2 v[8];
#pragma unroll
  for (int j = 0; j < 8; ++j) v[j] = buf[lane + 64 * j];
  fft512_fwd(v, buf, tw, lane);
  TS(2);
  if (kd != 0) {
#pragma unroll
    for (int k2 = 0; k2 < 8; ++k2) v[k2] = cmul(v[k2], unpk(wv[k2]));
  } else {
    f32x4* ex = (f32x4*)(lds + 4 * PS);
#pragma unroll
    for (int k2 = 0; k2 < 8; ++k2) {
      f32x4 e; e.xy = v[k2]; e.z = U2F(wv[k2]); e.w = U2F(wx[k2]);
      ex[kbase + 256 * k2] = e;
    }
    SYNC();
#pragma unroll
    for (int k2 = 0; k2 < 8; ++k2) {
      const int k = kbase + 256 * k2, kn = (2048 - k) & 2047;
      const f32x4 e = ex[kn];
      const f32x2 c1 = v[k2], c2 = e.xy;
      f32x2 r0, r1;
      r0.x = 0.5f * (c1.x + c2.x); r0.y = 0.5f * (c1.y - c2.y);
      r1.x = 0.5f * (c1.y + c2.y); r1.y = 0.5f * (c2.x - c1.x);
      const u32x4 eu = __builtin_bit_cast(u32x4, e);
      const u32 ez = eu.z, ew = eu.w;
      const f32x2 a1 = unpk(wv[k2]), a2 = unpk(ez), b1 = unpk(wx[k2]), b2 = unpk(ew);
      f32x2 h0, h1;
      h0.x = 0.5f * (a1.x + a2.x); h0.y = 0.5f * (a1.y - a2.y);
      h1.x = 0.5f * (b1.x + b2.x); h1.y = 0.5f * (b1.y - b2.y);
      const f32x2 p0 = cmul(r0, h0), p1 = cmul(r1, h1);
      v[k2].x = p0.x - p1.y;
      v[k2].y = p0.y + p1.x;
    }
  }
  fft512_inv(v, buf, tw, lane);
  TS(3);
  wave_sync();
#pragma unroll
  for (int j = 0; j < 8; ++j) buf[lane + 64 * j] = v[j];
  SYNC();
  {
    f32x2 y[2][4];
#pragma unroll
    for (int k = 0; k < 4; ++k) {
      const f32x4 o = *(const f32x4*)(lds + k * PS + 2 * tid);
      y[0][k] = o.xy; y[1][k] = o.zw;
      if (k) { y[0][k] = cmulc(y[0][k], t4[0][k - 1]); y[1][k] = cmulc(y[1][k], t4[1][k - 1]); }
    }
    f32x2 o[2][4];
    dft4<1>(y[0][0], y[0][1], y[0][2], y[0][3], o[0][0], o[0][1], o[0][2], o[0][3]);
    dft4<1>(y[1][0], y[1][1], y[1][2], y[1][3], o[1][0], o[1][1], o[1][2], o[1][3]);
    const rsrc_t rw2 = mkrsrc(ws);
#pragma unroll
    for (int n3 = 0; n3 < 4; ++n3) {
      f32x4 w; w.xy = o[0][n3]; w.zw = o[1][n3];
      st16(rw2, (u32)WS_ZT + (u32)(kd * SEQ * 8 + (tid + 256 * n3) * 16), __builtin_bit_cast(u32x4, w));
    }
  }
  WAIT_V(0);
  SYNC();
  u32* sync = (u32*)(ws + WS_SYNC);
  if (tid == 0) {
    if (__hip_atomic_fetch_add(sync + (kd & 7) * 16, 1u, __ATOMIC_RELAXED, __HIP_MEMORY_SCOPE_AGENT) == 31)
      __hip_atomic_fetch_add(sync + 40 * 16, 1u, __ATOMIC_RELAXED, __HIP_MEMORY_SCOPE_AGENT);
  }
  TS(4);

  const int s0 = kd * 8;
  const int row = s0 + 2 * wave;
  const float g0 = ((const float*)(ws + WS_G))[row], g1 = ((const float*)(ws + WS_G))[row + 1];
  if (wave == 0) {
    for (int it = 0; it < (1 << 22); ++it) {
      if (__hip_atomic_load(sync + 40 * 16, __ATOMIC_RELAXED, __HIP_MEMORY_SCOPE_AGENT) >= 8) break;
      __builtin_amdgcn_s_sleep(4);
    }
  }
  SYNC();
  TS(5);
  const rsrc_t rws = mkrsrc(ws);
  u32x4 z[4];
#pragma unroll
  for (int r = 0; r < 4; ++r) {
    const int i = tid + 256 * r;
    z[r] = __builtin_amdgcn_raw_buffer_load_b128(rws, (u32)WS_ZT + (u32)((i >> 2) * SEQ + s0 + 2 * (i & 3)) * 8, 0, 16);
  }
#pragma unroll
  for (int r = 0; r < 4; ++r) {
    const int i = tid + 256 * r;
    const int kk = i >> 2, p = i & 3;
    const f32x4 qq = __builtin_bit_cast(f32x4, z[r]);
    f32x2 c1, c2;
    if (kk) {
      c1.x = qq.x - qq.w; c1.y = qq.y + qq.z;
      c2.x = qq.x + qq.w; c2.y = qq.z - qq.y;
    } else {
      c1.x = qq.x; c1.y = qq.z;
      c2.x = qq.y; c2.y = qq.w;
    }
    lds[p * PS3 + sslot(kk)] = c1;
    lds[p * PS3 + sslot(kk ? 512 - kk : 256)] = c2;
  }
  SYNC();
  TS(6);
  f32x2* buf3 = lds + wave * PS3;
#pragma unroll
  for (int k2 = 0; k2 < 8; ++k2) v[k2] = buf3[sslot(hi + 8 * lo + 64 * k2)];
  fft512_inv(v, buf3, tw, lane);
  wave_sync();
#pragma unroll
  for (int j = 0; j < 8; ++j) buf3[lane + 2 * (lane >> 3) + 80 * j] = v[j];
  wave_sync();
  f32x4 u[4];
#pragma unroll
  for (int j = 0; j < 4; ++j) u[j] = *(const f32x4*)(buf3 + 10 * lane + 2 * j);
  TS(7);
  for (int it = 0; it < (1 << 22); ++it) {
    if (__hip_atomic_load(sync + (8 + (s0 >> 6)) * 16, __ATOMIC_RELAXED, __HIP_MEMORY_SCOPE_AGENT) >= 8) break;
    __builtin_amdgcn_s_sleep(2);
  }
  asm volatile("" ::: "memory");
  TS(8);
  const u32x4 b0 = __builtin_amdgcn_raw_buffer_load_b128(rws, (u32)WS_B + (u32)(row * DM + lane * 8) * 2, 0, 16);
  const u32x4 b1 = __builtin_amdgcn_raw_buffer_load_b128(rws, (u32)WS_B + (u32)((row + 1) * DM + lane * 8) * 2, 0, 16);
  const float sc = 1.0f / (2048.0f * 512.0f);
  const float og0 = bfr(1.0f - g0), og1 = bfr(1.0f - g1);
  u32x4 o0, o1;
#pragma unroll
  for (int j = 0; j < 4; ++j) {
    const f32x2 bv0 = unpk(b0[j]), bv1 = unpk(b1[j]);
    o0[j] = pack2(bfr(g0 * bfr(u[j].x * sc)) + bfr(og0 * bv0.x), bfr(g0 * bfr(u[j].z * sc)) + bfr(og0 * bv0.y));
    o1[j] = pack2(bfr(g1 * bfr(u[j].y * sc)) + bfr(og1 * bv1.x), bfr(g1 * bfr(u[j].w * sc)) + bfr(og1 * bv1.y));
  }
  const rsrc_t ro = mkrsrc(out);
  st16(ro, (u32)(row * DM + lane * 8) * 2, o0);
  st16(ro, (u32)((row + 1) * DM + lane * 8) * 2, o1);
  TS(9);
}

NOPK __global__ __launch_bounds__(256, 2) void k_l2(char* __restrict__ ws, u16* __restrict__ out) {
  extern __shared__ __attribute__((aligned(16))) char smem[];
  if (BIDX >= 504) {
    const u32 d = code_prefetch(TIDX);
    WAIT_V(0);
    asm volatile("" ::"v"(d));
  }
  if (BIDX >= 256) g2_body(ws, smem, BIDX - 256);
  else k2_body(ws, smem, BIDX, out);
}

extern "C" void kernel_launch(void* const* d_in, const int* in_sizes, int n_in, void* d_out, int out_size,
                              void* d_ws, size_t ws_size, hipStream_t stream) {
  const u16* x = (const u16*)d_in[0];
  const u16* wr = (const u16*)d_in[1];
  const u16* wi = (const u16*)d_in[2];
  const u16* wg = (const u16*)d_in[4];
  const u16* wu = (const u16*)d_in[5];
  const u16* wd = (const u16*)d_in[6];
  const u16* gw = (const u16*)d_in[7];
  char* ws = (char*)d_ws;
  (void)hipFuncSetAttribute((const void*)k_l1, hipFuncAttributeMaxDynamicSharedMemorySize, L1_LDS);
  (void)hipFuncSetAttribute((const void*)k_l2, hipFuncAttributeMaxDynamicSharedMemorySize, L2_LDS);
  k_l1<<<256, 1024, L1_LDS, stream>>>(x, wr, wi, wg, wu, wd, gw, ws);
  k_l2<<<512, 256, L2_LDS, stream>>>(ws, (u16*)d_out);
}
```

```cpp
#include <hip/hip_runtime.h>
#include <hip/hip_bf16.h>
#include <stdint.h>

#if defined(__HIP_DEVICE_COMPILE__)
#define NOPK __attribute__((target("no-packed-fp32-ops")))
#else
#define NOPK
#endif
#define DEVI NOPK __device__ __forceinline__
#define TS(i)

typedef __attribute__((ext_vector_type(8))) short bf16x8;
typedef __attribute__((ext_vector_type(4))) short bf16x4;
typedef __attribute__((ext_vector_type(4))) float f32x4;
typedef __attribute__((ext_vector_type(2))) float f32x2;
typedef unsigned short u16;
typedef unsigned int u32;
typedef __attribute__((ext_vector_type(4))) unsigned int u32x4;
typedef __attribute__((ext_vector_type(2))) unsigned int u32x2;
typedef __attribute__((address_space(3))) bf16x4* lds_b4_ptr;
#define TIDX ((int)__builtin_amdgcn_workitem_id_x())
#define BIDX ((int)__builtin_amdgcn_workgroup_id_x())
#define U2F(x) __builtin_bit_cast(float, (u32)(x))
#define SYNC() do { __builtin_amdgcn_fence(__ATOMIC_RELEASE, "workgroup"); __builtin_amdgcn_s_barrier(); \
                    __builtin_amdgcn_fence(__ATOMIC_ACQUIRE, "workgroup"); } while (0)
DEVI float fexp(float x) { return __builtin_amdgcn_exp2f(x * 1.44269504088896340736f); }
DEVI float shfl_xor(float v, int m, int lane) {
  return __builtin_bit_cast(float, __builtin_amdgcn_ds_bpermute((lane ^ m) << 2, __builtin_bit_cast(int, v)));
}

constexpr int SEQ = 2048, DM = 512, DF = 257, FF = 1376;
constexpr int KS32 = FF / 32;

constexpr size_t WS_YT = 0;
constexpr size_t WS_WT = WS_YT + (size_t)DF * SEQ * 8;
constexpr size_t WS_H = WS_WT + (size_t)DF * SEQ * 4;
constexpr size_t WS_WD = WS_H + (size_t)SEQ * FF * 2;
constexpr size_t WS_B = WS_WD + (size_t)FF * DM * 2;
constexpr size_t WS_G = WS_B + (size_t)SEQ * DM * 2;
constexpr size_t WS_SYNC = WS_G + 8192;
constexpr size_t WS_ZT = WS_SYNC + 4096;

DEVI float bf2f(u16 v) { return U2F(((u32)v) << 16); }
DEVI u16 f2bf(float f) {
  __bf16 b = (__bf16)f;
  return __builtin_bit_cast(u16, b);
}
DEVI float bfr(float f) { return bf2f(f2bf(f)); }

DEVI f32x2 cmul(f32x2 a, f32x2 b) {
  f32x2 r;
  r.x = a.x * b.x - a.y * b.y;
  r.y = a.x * b.y + a.y * b.x;
  return r;
}
DEVI f32x2 cmulc(f32x2 a, f32x2 b) {
  f32x2 r;
  r.x = a.x * b.x + a.y * b.y;
  r.y = a.y * b.x - a.x * b.y;
  return r;
}
DEVI f32x2 twid(float frac) {
  f32x2 r;
  r.x = __builtin_amdgcn_cosf(frac);
  r.y = -__builtin_amdgcn_sinf(frac);
  return r;
}

template <int SGN>
DEVI f32x2 mul_i(f32x2 z) {
  f32x2 r;
  if (SGN < 0) { r.x = z.y; r.y = -z.x; } else { r.x = -z.y; r.y = z.x; }
  return r;
}
template <int SGN>
DEVI void dft4(f32x2 b0, f32x2 b1, f32x2 b2, f32x2 b3, f32x2& y0, f32x2& y1, f32x2& y2,
                                     f32x2& y3) {
  f32x2 c0 = b0 + b2, c1 = b0 - b2, c2 = b1 + b3, c3 = mul_i<SGN>(b1 - b3);
  y0 = c0 + c2; y2 = c0 - c2; y1 = c1 + c3; y3 = c1 - c3;
}
template <int SGN>
DEVI void dft8(f32x2 (&v)[8]) {
  const float R = 0.70710678118654752440f;
  f32x2 a0 = v[0] + v[4], a4 = v[0] - v[4];
  f32x2 a1 = v[1] + v[5], a5 = v[1] - v[5];
  f32x2 a2 = v[2] + v[6], a6 = v[2] - v[6];
  f32x2 a3 = v[3] + v[7], a7 = v[3] - v[7];
  f32x2 t5, t7;
  if (SGN < 0) {
    t5.x = (a5.x + a5.y) * R; t5.y = (a5.y - a5.x) * R;
    t7.x = (a7.y - a7.x) * R; t7.y = -(a7.x + a7.y) * R;
  } else {
    t5.x = (a5.x - a5.y) * R; t5.y = (a5.x + a5.y) * R;
    t7.x = -(a7.x + a7.y) * R; t7.y = (a7.x - a7.y) * R;
  }
  f32x2 t6 = mul_i<SGN>(a6);
  dft4<SGN>(a0, a1, a2, a3, v[0], v[2], v[4], v[6]);
  dft4<SGN>(a4, t5, t6, t7, v[1], v[3], v[5], v[7]);
}

DEVI void wave_sync() {
  asm volatile("s_waitcnt lgkmcnt(0)" ::: "memory");
  __builtin_amdgcn_wave_barrier();
}

struct Tw512 {
  f32x2 a[7];
  f32x2 b[7];
};
DEVI void make_tw512(Tw512& t, int lane) {
#pragma unroll
  for (int k = 1; k < 8; ++k) {
    t.a[k - 1] = twid((float)((lane * k) & 511) * (1.0f / 512.0f));
    t.b[k - 1] = twid((float)(((lane & 7) * k) & 63) * (1.0f / 64.0f));
  }

}

DEVI void fft512_fwd(f32x2 (&v)[8], f32x2* buf, const Tw512& tw, int lane) {
  const int hi = lane >> 3, lo = lane & 7;
  dft8<-1>(v);
#pragma unroll
  for (int k = 1; k < 8; ++k) v[k] = cmul(v[k], tw.a[k - 1]);
  wave_sync();
#pragma unroll
  for (int k = 0; k < 8; ++k) buf[72 * k + lane] = v[k];
  wave_sync();
#pragma unroll
  for (int n = 0; n < 8; ++n) v[n] = buf[72 * hi + 8 * n + lo];
  dft8<-1>(v);
#pragma unroll
  for (int k = 1; k < 8; ++k) v[k] = cmul(v[k], tw.b[k - 1]);
  wave_sync();
#pragma unroll
  for (int k = 0; k < 8; ++k) buf[72 * hi + 9 * k + lo] = v[k];
  wave_sync();
#pragma unroll
  for (int n = 0; n < 8; ++n) v[n] = buf[72 * hi + 9 * lo + n];
  dft8<-1>(v);
}
DEVI void fft512_inv(f32x2 (&v)[8], f32x2* buf, const Tw512& tw, int lane) {
  const int hi = lane >> 3, lo = lane & 7;
  dft8<1>(v);
  wave_sync();
#pragma unroll
  for (int n = 0; n < 8; ++n) buf[72 * hi + 9 * lo + n] = v[n];
  wave_sync();
#pragma unroll
  for (int k = 0; k < 8; ++k) v[k] = buf[72 * hi + 9 * k + lo];
#pragma unroll
  for (int k = 1; k < 8; ++k) v[k] = cmulc(v[k], tw.b[k - 1]);
  dft8<1>(v);
  wave_sync();
#pragma unroll
  for (int n = 0; n < 8; ++n) buf[72 * hi + 8 * n + lo] = v[n];
  wave_sync();
#pragma unroll
  for (int k = 0; k < 8; ++k) v[k] = buf[72 * k + lane];
#pragma unroll
  for (int k = 1; k < 8; ++k) v[k] = cmulc(v[k], tw.a[k - 1]);
  dft8<1>(v);
}

DEVI int sslot(int k) { return k + 4 * (k >> 5); }
constexpr int PS = 584;

#define PREFETCH 0
#define AUX_GATE 7
constexpr int G1_NS = 3;
constexpr int G1_X_OFF = G1_NS * 24576;
constexpr int AX_OFF = G1_X_OFF + G1_NS * 16384;
constexpr int AX_W_OFF = AX_OFF + 4 * PS * 8;
constexpr int AX_WD_OFF = AX_W_OFF + 10240;
constexpr int AX_GW_OFF = AX_WD_OFF;
constexpr int L1_CNT_OFF = AX_WD_OFF + 8192;
constexpr int L1_LDS = L1_CNT_OFF + 256;

#define WAIT_V(n) asm volatile("s_waitcnt vmcnt(" #n ")" ::: "memory")
#define WAIT_L(n) asm volatile("s_waitcnt lgkmcnt(" #n ")" ::: "memory")
#define RAW_BAR() asm volatile("s_barrier" ::: "memory")

typedef __amdgpu_buffer_rsrc_t rsrc_t;
typedef __attribute__((ext_vector_type(4))) int i32x4;
DEVI void dma16(const void* gsrc, u32 lds_dst) {
  u32 keep;
  asm volatile("s_mov_b32 %0, m0\n\ts_mov_b32 m0, %2\n\ts_nop 0\n\tglobal_load_lds_dwordx4 %1, off\n\ts_mov_b32 m0, %0"
               : "=&s"(keep) : "v"(gsrc), "s"(lds_dst) : "memory");
}
DEVI void dma16b(i32x4 rs, u32 voff, u32 soff, u32 lds_dst) {
  u32 keep;
  asm volatile("s_mov_b32 %0, m0\n\ts_mov_b32 m0, %4\n\ts_nop 0\n\tbuffer_load_dwordx4 %1, %2, %3 offen lds\n\ts_mov_b32 m0, %0"
               : "=&s"(keep) : "v"(voff), "s"(rs), "s"(soff), "s"(lds_dst) : "memory");
}
DEVI i32x4 mkrs(const void* p, u32 bytes) {
  const unsigned long long a = (unsigned long long)p;
  i32x4 r;
  r.x = __builtin_amdgcn_readfirstlane((int)(u32)a);
  r.y = __builtin_amdgcn_readfirstlane((int)(u32)(a >> 32) & 0xffff);
  r.z = (int)bytes;
  r.w = 0x00020000;
  return r;
}
DEVI rsrc_t mkrsrc(const void* p) { return __builtin_amdgcn_make_buffer_rsrc((void*)p, 0, 0x7fffffff, 0x00020000); }
DEVI void st16(rsrc_t r, u32 off, u32x4 v) { __builtin_amdgcn_raw_buffer_store_b128(v, r, off, 0, 16); }
DEVI u32 pack2(float a, float b) { return (u32)f2bf(a) | ((u32)f2bf(b) << 16); }

typedef __attribute__((address_space(3))) u32* lds_u32_ptr;
DEVI void lds_signal(u32 addr, int lane) {
  unsigned long long keep;
  asm volatile("s_mov_b64 %0, exec\n\ts_mov_b64 exec, 1\n\tds_add_u32 %1, %2\n\ts_mov_b64 exec, %0"
               : "=&s"(keep) : "v"(addr), "v"(1u) : "memory");
}
DEVI void lds_wait1(u32 addr, u32 need) {
  while (true) {
    u32 v;
    asm volatile("ds_read_b32 %0, %1\n\ts_waitcnt lgkmcnt(0)" : "=v"(v) : "v"(addr) : "memory");
    if ((u32)__builtin_amdgcn_readfirstlane(v) >= need) break;
    __builtin_amdgcn_s_sleep(1);
  }
}
DEVI void lds_wait2(u32 addr, u32 need0, u32 need1) {
  while (true) {
    u32x2 v;
    asm volatile("ds_read_b64 %0, %1\n\ts_waitcnt lgkmcnt(0)" : "=v"(v) : "v"(addr) : "memory");
    const u32 a = __builtin_amdgcn_readfirstlane(v.x), b = __builtin_amdgcn_readfirstlane(v.y);
    if (a >= need0 && b >= need1) break;
    __builtin_amdgcn_s_sleep(1);
  }
}

DEVI void aux_body(const u16* __restrict__ x, const u16* __restrict__ wr, const u16* __restrict__ wi,
                   const u16* __restrict__ wd, const u16* __restrict__ gw, char* __restrict__ ws, char* smem, int a,
                   int wave, int lane, bool gated) {
  constexpr int TSK = 0;
  const u32 lds0 = (u32)(size_t)(__attribute__((address_space(3))) char*)smem;
  const rsrc_t rws = mkrsrc(ws);
  const int s0 = a * 8;
  const int tid = wave * 64 + lane;
  f32x2* aux = (f32x2*)(smem + AX_OFF);
  {
    const int p = wave;
    Tw512 tw;
    make_tw512(tw, lane);
    WAIT_V(0);
    TS(1);
    const u16* xa = (const u16*)(aux + p * PS);
    const u16* gl = (const u16*)(smem + AX_GW_OFF + p * 1024);
    u16 ra[8], rb[8], rg[8];
#pragma unroll
    for (int j = 0; j < 8; ++j) {
      ra[j] = xa[lane + 64 * j];
      rb[j] = xa[DM + lane + 64 * j];
      rg[j] = gl[lane + 64 * j];
    }
    f32x2 v[8];
    float sa = 0.f, sb = 0.f;
#pragma unroll
    for (int j = 0; j < 8; ++j) {
      v[j].x = bf2f(ra[j]);
      v[j].y = bf2f(rb[j]);
      float w = bf2f(rg[j]);
      sa += bfr(v[j].x * w);
      sb += bfr(v[j].y * w);
    }
#pragma unroll
    for (int o = 32; o > 0; o >>= 1) {
      sa += shfl_xor(sa, o, lane);
      sb += shfl_xor(sb, o, lane);
    }
    if (lane < 2) {
      float s = bfr(lane ? sb : sa);
      float gg = bfr(1.0f / (1.0f + fexp(-s)));
      ((float*)(ws + WS_G))[s0 + 2 * p + lane] = gg;
    }
    f32x2* buf = aux + p * PS;
    fft512_fwd(v, buf, tw, lane);
    wave_sync();
    const int hi = lane >> 3, lo = lane & 7;
#pragma unroll
    for (int k2 = 0; k2 < 8; ++k2) buf[sslot(hi + 8 * lo + 64 * k2)] = v[k2];
  }
  WAIT_L(0);
  lds_signal(lds0 + L1_CNT_OFF + 42 * 4, lane);
  TS(2);
  if (gated) lds_wait1(lds0 + L1_CNT_OFF + 44 * 4, 4);
  {
    const int p = wave;
    const char* pr = (const char*)wr + (size_t)a * 4112;
    const char* pi = (const char*)wi + (size_t)a * 4112;
    dma16(pr + p * 1024 + lane * 16, lds0 + AX_W_OFF + p * 1024);
    dma16(pi + p * 1024 + lane * 16, lds0 + AX_W_OFF + 5120 + p * 1024);
    if (p < 2) dma16((p ? pi : pr) + (lane == 0 ? 4096 : 0), lds0 + AX_W_OFF + p * 5120 + 4096);
    const char* pd = (const char*)wd + (size_t)(a < 172 ? a : 0) * 8192;
    dma16(pd + p * 1024 + lane * 16, lds0 + AX_WD_OFF + p * 1024);
    dma16(pd + 4096 + p * 1024 + lane * 16, lds0 + AX_WD_OFF + 4096 + p * 1024);
  }
  lds_wait1(lds0 + L1_CNT_OFF + 42 * 4, 4);
#pragma unroll
  for (int r = 0; r < 4; ++r) {
    const int i = tid + 256 * r;
    const int kd = i >> 2, p = i & 3;
    const f32x2 z1 = aux[p * PS + sslot(kd)];
    const f32x2 z2 = aux[p * PS + sslot(kd ? 512 - kd : 256)];
    f32x4 o;
    if (kd) {
      o.x = 0.5f * (z1.x + z2.x);
      o.y = 0.5f * (z1.y - z2.y);
      o.z = 0.5f * (z1.y + z2.y);
      o.w = 0.5f * (z2.x - z1.x);
    } else {
      o.x = z1.x; o.y = z2.x; o.z = z1.y; o.w = z2.y;
    }
    st16(rws, (u32)WS_YT + (u32)(kd * SEQ + s0 + 2 * p) * 8, __builtin_bit_cast(u32x4, o));
  }
  TS(3);
  WAIT_V(4);
  lds_signal(lds0 + L1_CNT_OFF + 43 * 4, lane);
  lds_wait1(lds0 + L1_CNT_OFF + 43 * 4, 4);
  {
    const u16* lr = (const u16*)(smem + AX_W_OFF);
    const u16* li = (const u16*)(smem + AX_W_OFF + 5120);
#pragma unroll
    for (int r = 0; r < 3; ++r) {
      const int i = tid + 256 * r;
      if (i < DF * 2) {
        const int kd = i >> 1, h = i & 1;
        u32x4 o;
#pragma unroll
        for (int q = 0; q < 4; ++q) o[q] = (u32)lr[(h * 4 + q) * DF + kd] | ((u32)li[(h * 4 + q) * DF + kd] << 16);
        st16(rws, (u32)WS_WT + (u32)(kd * SEQ + s0 + h * 4) * 4, o);
      }
    }
  }
  if (a < 172) {
    const u16* ld = (const u16*)(smem + AX_WD_OFF);
#pragma unroll
    for (int r = 0; r < 2; ++r) {
      const int n = tid + 256 * r;
      u32x4 o;
#pragma unroll
      for (int q = 0; q < 4; ++q) o[q] = (u32)ld[(2 * q) * DM + n] | ((u32)ld[(2 * q + 1) * DM + n] << 16);
      const int ks = a >> 2, kq = a & 3;
      st16(rws, (u32)WS_WD + (u32)(((ks * 32 + (n >> 4)) * 64 + (n & 15) + 16 * kq) * 16), o);
    }
  }
  TS(4);
}

DEVI u32 code_prefetch(int i, int n64) {
  const char* pc = (const char*)(__builtin_amdgcn_s_getpc() & ~127ull);
  u32 d;
  asm volatile("global_load_dword %0, %1, off" : "=v"(d) : "v"(pc + (i < n64 ? i : 0) * 64) : "memory");
  return d;
}
NOPK __global__ __launch_bounds__(1024, 4) void k_l1(const u16* __restrict__ x, const u16* __restrict__ wr,
                                                const u16* __restrict__ wi, const u16* __restrict__ wg,
                                                const u16* __restrict__ wu, const u16* __restrict__ wd,
                                                const u16* __restrict__ gw, char* __restrict__ ws) {
  extern __shared__ __attribute__((aligned(16))) char smem[];
  const int bid = BIDX;
  const int tid = TIDX, lane = tid & 63;
  const int wave = __builtin_amdgcn_readfirstlane(tid >> 6);
  constexpr int TSK = 0;
  TS(0);
  const u32 lds0 = (u32)(size_t)(__attribute__((address_space(3))) char*)smem;
  const u32 cnt = lds0 + L1_CNT_OFF;
  if (bid >= 240 && wave < 8) {
    const u32 d = code_prefetch(tid, 456);
    WAIT_V(0);
    asm volatile("" ::"v"(d));
  }
  if (wave == 0) { ((u32*)(smem + L1_CNT_OFF))[lane] = 0; WAIT_L(0); }
  RAW_BAR();
  const int xcd = bid & 7, idx = bid >> 3;
  const int T = xcd * 30 + (idx < 30 ? idx : 0);
  const int fg = T / 80, rem = T - fg * 80;
  const int mt_ = rem / 5, fi = rem - mt_ * 5;
  const int m0 = mt_ * 128, f0 = (fg * 5 + fi) * 96;
  if (wave >= 12) {
    __builtin_amdgcn_s_setprio(2);
    const int w = wave - 12;
    if (bid == 255 && w == 0) ((u32*)(ws + WS_SYNC))[lane * 16] = 0;
    const char* px = (const char*)x + (size_t)(bid * 8 + 2 * w) * 1024;
    dma16(px + lane * 16, lds0 + AX_OFF + w * (PS * 8));
    dma16(px + 1024 + lane * 16, lds0 + AX_OFF + w * (PS * 8) + 1024);
    dma16((const char*)gw + lane * 16, lds0 + AX_GW_OFF + w * 1024);
    aux_body(x, wr, wi, wd, gw, ws, smem, bid, w, lane, idx < 30);
    return;
  }
  if (idx >= 30) return;

  {
    const int q4 = wave >= 8 ? wave - 8 : (wave >> 1);
    const i32x4 rx = mkrs(x, SEQ * DM * 2), rg = mkrs(wg, DM * FF * 2), ru = mkrs(wu, DM * FF * 2);
    u32 vx, vw[3];
    {
      const int row = lane >> 3;
      const int c = (lane & 7) ^ row;
      vx = (u32)((m0 + q4 * 32 + row) * DM + c * 8) * 2;
#pragma unroll
      for (int j = 0; j < 3; ++j) {
        const int q = 64 * j + lane;
        const int k = q / 12, cp = q - k * 12;
        const int c2 = cp ^ (((k >> 3) & 1) << 1);
        vw[j] = (u32)((q4 * 16 + k) * FF + f0 + c2 * 8) * 2;
      }
    }
    auto issue = [&](int s, int lo, int hi) NOPK __attribute__((always_inline)) {
      const int slot = s % G1_NS;
      const u32 wdst = lds0 + slot * 24576 + q4 * 3072, xdst = lds0 + G1_X_OFF + slot * 16384 + q4 * 4096;
      const u32 sw = s * (64 * FF * 2);
#pragma unroll
      for (int n = 0; n < 10; ++n) {
        if (n >= lo && n < hi) {
          if (n < 3) dma16b(rg, vw[n], sw, wdst + n * 1024);
          else if (n < 7) dma16b(rx, vx, s * 128 + (n - 3) * 8192, xdst + (n - 3) * 1024);
          else dma16b(ru, vw[n - 7], sw, wdst + 12288 + (n - 7) * 1024);
        }
      }
    };
    if (wave >= 8) {
      issue(1, 0, 10);
      issue(2, 0, 5);
      WAIT_V(5);
      lds_signal(cnt + 1 * 4, lane);
      issue(2, 5, 10);
#pragma unroll 1
      for (int s = 3; s < 8; ++s) {
        lds_wait1(cnt + (16 + s - 3) * 4, 8);
        issue(s, 0, 3);
        WAIT_V(3);
        lds_signal(cnt + (s - 1) * 4, lane);
        issue(s, 3, 10);
        if (s == AUX_GATE) lds_signal(cnt + 44 * 4, lane);
      }
      WAIT_V(0);
      lds_signal(cnt + 7 * 4, lane);
      return;
    }
    if (wave & 1) issue(0, 5, 10); else issue(0, 0, 5);
    WAIT_V(0);
    lds_signal(cnt, lane);
  }

  const int wm = wave >> 1, wf = wave & 1;
  const int l15 = lane & 15, lq = lane >> 4;
  int xoff[2][2];
#pragma unroll
  for (int mt = 0; mt < 2; ++mt)
#pragma unroll
    for (int kk = 0; kk < 2; ++kk) {
      const int row = wm * 32 + mt * 16 + l15;
      xoff[mt][kk] = G1_X_OFF + row * 128 + (((kk * 4 + lq) ^ (row & 7)) << 4);
    }
  const int wq = l15 >> 2, wp = l15 & 3;
  int woff[3];
#pragma unroll
  for (int ft = 0; ft < 3; ++ft)
    woff[ft] = (8 * lq + wq) * 192 + (((2 * (wf * 3 + ft) + (wp >> 1)) ^ (2 * (lq & 1))) << 4) + (wp & 1) * 8;

  f32x4 accg[3][2], accu[3][2];
#pragma unroll
  for (int a = 0; a < 3; ++a)
#pragma unroll
    for (int b = 0; b < 2; ++b) { accg[a][b] = (f32x4)0.f; accu[a][b] = (f32x4)0.f; }

  bf16x8 xa[2], xb[2], wgf[3], wuf[3];
  auto rdx = [&](bf16x8(&xf)[2], int xs, int kk) NOPK __attribute__((always_inline)) {
#pragma unroll
    for (int mt = 0; mt < 2; ++mt) xf[mt] = *(const bf16x8*)(smem + xs + xoff[mt][kk]);
  };
  auto rdw = [&](bf16x8(&wfr)[3], int wsl, int kk, int mat) NOPK __attribute__((always_inline)) {
#pragma unroll
    for (int n = 0; n < 3; ++n) {
      const char* base = smem + (wsl + woff[n]) + kk * 6144 + mat * 12288;
      bf16x4 lo = __builtin_amdgcn_ds_read_tr16_b64_v4i16((lds_b4_ptr)(base));
      bf16x4 hi = __builtin_amdgcn_ds_read_tr16_b64_v4i16((lds_b4_ptr)(base + 768));
      wfr[n] = __builtin_shufflevector(lo, hi, 0, 1, 2, 3, 4, 5, 6, 7);
    }
  };
  auto mma = [&](f32x4(&acc)[3][2], bf16x8(&xf)[2], bf16x8(&wfr)[3]) NOPK __attribute__((always_inline)) {
#pragma unroll
    for (int ft = 0; ft < 3; ++ft)
#pragma unroll
      for (int mt = 0; mt < 2; ++mt)
        acc[ft][mt] = __builtin_amdgcn_mfma_f32_16x16x32_bf16(wfr[ft], xf[mt], acc[ft][mt], 0, 0, 0);
  };
  auto interleave = [&]() NOPK __attribute__((always_inline)) {
#pragma unroll
    for (int i = 0; i < 6; ++i) {
      __builtin_amdgcn_sched_group_barrier(0x008, 1, 0);
      __builtin_amdgcn_sched_group_barrier(0x100, 2, 0);
    }
  };
  const volatile u32* cv = (const volatile u32*)(smem + L1_CNT_OFF);

  TS(1);
  lds_wait1(cnt, 8);
  TS(4);
  rdx(xa, 0, 0);
  rdw(wgf, 0, 0, 0);
#define SB() __builtin_amdgcn_sched_barrier(0)
  int slot = 0;
#pragma unroll 1
  for (int S = 0; S < 8; ++S) {
    const int wsl = slot * 24576, xs = slot * 16384;
    slot = slot == G1_NS - 1 ? 0 : slot + 1;
    const int wsn = slot * 24576, xn = slot * 16384;
    SB(); rdw(wuf, wsl, 0, 1); mma(accg, xa, wgf); interleave();
    SB(); rdw(wgf, wsl, 1, 0); rdx(xb, xs, 1); mma(accu, xa, wuf); interleave();
    SB(); rdw(wuf, wsl, 1, 1); const u32 fv = cv[S + 1]; mma(accg, xb, wgf); interleave();
    SB();
    WAIT_L(0);
    lds_signal(cnt + (16 + S) * 4, lane);
    if (S < 7) {
      if ((u32)__builtin_amdgcn_readfirstlane(fv) < 4) lds_wait1(cnt + (S + 1) * 4, 4);
    }
    SB();
    rdw(wgf, wsn, 0, 0); rdx(xa, xn, 0);
    mma(accu, xb, wuf);
    interleave();
  }
  SB();
  TS(5);
  const rsrc_t rws = mkrsrc(ws);
  const int lane2 = __builtin_amdgcn_mbcnt_hi(~0u, __builtin_amdgcn_mbcnt_lo(~0u, 0u));
  const int e15 = lane2 & 15, eq = lane2 >> 4;
#pragma unroll
  for (int ft = 0; ft < 3; ++ft) {
    u32 pk[2][2];
#pragma unroll
    for (int mt = 0; mt < 2; ++mt) {
      float o[4];
#pragma unroll
      for (int r = 0; r < 4; ++r) {
        const float g = bfr(accg[ft][mt][r]);
        const float sl = bfr(g * __builtin_amdgcn_rcpf(1.0f + fexp(-g)));
        const float u = bfr(accu[ft][mt][r]);
        o[r] = sl * u;
      }
      pk[mt][0] = pack2(o[0], o[1]);
      pk[mt][1] = pack2(o[2], o[3]);
    }
    auto s0_ = __builtin_amdgcn_permlane16_swap(pk[0][0], pk[1][0], false, false);
    auto s1_ = __builtin_amdgcn_permlane16_swap(pk[0][1], pk[1][1], false, false);
    u32x4 ov;
    ov.x = s0_[0]; ov.y = s1_[0]; ov.z = s0_[1]; ov.w = s1_[1];
    const int f = f0 + (wf * 3 + ft) * 16 + 8 * (eq >> 1);
    const int m = m0 + wm * 32 + (eq & 1) * 16 + e15;
    if (f < FF)
      st16(rws, (u32)WS_H + (u32)(((m >> 4) * KS32 + (f >> 5)) * 1024 + ((m & 15) + 16 * ((f & 31) >> 3)) * 16), ov);
  }
  TS(6);
}

constexpr int L2_LDS = 4 * PS * 8 + 32768;
constexpr int G2_DEPTH = 4;

DEVI void g2_body(char* __restrict__ ws, char* smem, int bid) {
  const int tid = TIDX, lane = tid & 63;
  const int wave = __builtin_amdgcn_readfirstlane(tid >> 6);
  constexpr int TSK = 1;
  TS(0);
  const int xcd = bid & 7, idx = bid >> 3;
  const int mt64 = xcd * 4 + (idx >> 3), nt64 = idx & 7;
  const char* hb = ws + WS_H + (size_t)(mt64 * 4) * KS32 * 1024 + lane * 16;
  const char* wb = ws + WS_WD + (size_t)(nt64 * 4) * 1024 + lane * 16;
  f32x4 acc[4][4];
#pragma unroll
  for (int a = 0; a < 4; ++a)
#pragma unroll
    for (int b = 0; b < 4; ++b) acc[a][b] = (f32x4)0.f;
  constexpr int NS = G2_DEPTH + 1;
  bf16x8 hf[NS][4], wf[NS][4];
  auto ld = [&](int slot, int i) NOPK __attribute__((always_inline)) {
    int ks = wave + 4 * i;
    ks = ks > KS32 - 1 ? KS32 - 1 : ks;
#pragma unroll
    for (int j = 0; j < 4; ++j) {
      hf[slot][j] = *(const bf16x8*)(hb + ((size_t)j * KS32 + ks) * 1024);
      wf[slot][j] = *(const bf16x8*)(wb + ((size_t)ks * 32 + j) * 1024);
    }
  };
#pragma unroll
  for (int i = 0; i < G2_DEPTH; ++i) ld(i, i);
  __builtin_amdgcn_sched_barrier(0);
#pragma unroll
  for (int i = 0; i < 11; ++i) {
    if (i + G2_DEPTH < 11) ld((i + G2_DEPTH) % NS, i + G2_DEPTH);
    __builtin_amdgcn_sched_barrier(0);
    if (i < 10 || wave < 3) {
#pragma unroll
      for (int nt = 0; nt < 4; ++nt)
#pragma unroll
        for (int mt = 0; mt < 4; ++mt)
          acc[nt][mt] = __builtin_amdgcn_mfma_f32_16x16x32_bf16(wf[i % NS][nt], hf[i % NS][mt], acc[nt][mt], 0, 0, 0);
    }
    __builtin_amdgcn_sched_barrier(0);
  }
  TS(1);
  f32x4* red = (f32x4*)smem;
#pragma unroll
  for (int nt = 0; nt < 4; ++nt) {
    if (nt != wave) {
      const int src = wave < nt ? wave : wave - 1;
#pragma unroll
      for (int mt = 0; mt < 4; ++mt) red[((nt * 3 + src) * 4 + mt) * 64 + lane] = acc[nt][mt];
    }
  }
  SYNC();
  f32x4 fin[4];
#pragma unroll
  for (int nt = 0; nt < 4; ++nt)
    if (nt == wave) {
#pragma unroll
      for (int mt = 0; mt < 4; ++mt) fin[mt] = acc[nt][mt];
    }
  const rsrc_t rws = mkrsrc(ws);
  const int l15 = lane & 15, lq = lane >> 4;
#pragma unroll
  for (int mt = 0; mt < 4; ++mt)
#pragma unroll
    for (int s = 0; s < 3; ++s) fin[mt] += red[((wave * 3 + s) * 4 + mt) * 64 + lane];
#pragma unroll
  for (int pr = 0; pr < 2; ++pr) {
    auto s0_ = __builtin_amdgcn_permlane16_swap(pack2(fin[2 * pr][0], fin[2 * pr][1]),
                                                pack2(fin[2 * pr + 1][0], fin[2 * pr + 1][1]), false, false);
    auto s1_ = __builtin_amdgcn_permlane16_swap(pack2(fin[2 * pr][2], fin[2 * pr][3]),
                                                pack2(fin[2 * pr + 1][2], fin[2 * pr + 1][3]), false, false);
    u32x4 ov;
    ov.x = s0_[0]; ov.y = s1_[0]; ov.z = s0_[1]; ov.w = s1_[1];
    const int m = mt64 * 64 + (2 * pr + (lq & 1)) * 16 + l15;
    const int n = nt64 * 64 + wave * 16 + 8 * (lq >> 1);
    st16(rws, (u32)WS_B + (u32)(m * DM + n) * 2, ov);
  }
  WAIT_V(0);
  SYNC();
  if (tid == 0) __hip_atomic_fetch_add((u32*)(ws + WS_SYNC) + (8 + mt64) * 16, 1u, __ATOMIC_RELAXED, __HIP_MEMORY_SCOPE_AGENT);
  TS(2);
}

DEVI f32x2 unpk(u32 w) {
  f32x2 r;
  r.x = U2F(w << 16);
  r.y = U2F(w & 0xffff0000u);
  return r;
}

constexpr int PS3 = 648;
DEVI void k2_body(char* __restrict__ ws, char* smem, int kd, u16* __restrict__ out) {
  const int tid = TIDX, lane = tid & 63;
  const int wave = __builtin_amdgcn_readfirstlane(tid >> 6);
  constexpr int TSK = 1;
  TS(0);
  const f32x4* col = (const f32x4*)(ws + WS_YT + (size_t)kd * SEQ * 8);
  const u32* wt = (const u32*)(ws + WS_WT) + (size_t)kd * SEQ;
  f32x2* lds = (f32x2*)smem;
  f32x4 q[4];
#pragma unroll
  for (int n3 = 0; n3 < 4; ++n3) q[n3] = col[tid + 256 * n3];
  const int hi = lane >> 3, lo = lane & 7;
  const int kbase = wave + 4 * (hi + 8 * lo);
  u32 wv[8];
#pragma unroll
  for (int k2 = 0; k2 < 8; ++k2) wv[k2] = wt[kbase + 256 * k2];
  u32 wx[8];
  if (kd == 0) {
    const u32* wn = (const u32*)(ws + WS_WT) + (size_t)256 * SEQ;
#pragma unroll
    for (int k2 = 0; k2 < 8; ++k2) wx[k2] = wn[kbase + 256 * k2];
  }
  Tw512 tw;
  make_tw512(tw, lane);
  f32x2 t4[2][3];
#pragma unroll
  for (int h = 0; h < 2; ++h)
#pragma unroll
    for (int k = 1; k < 4; ++k) t4[h][k - 1] = twid((float)(((2 * tid + h) * k) & 2047) * (1.0f / 2048.0f));
  {
    f32x2 y[2][4];
    dft4<-1>(q[0].xy, q[1].xy, q[2].xy, q[3].xy, y[0][0], y[0][1], y[0][2], y[0][3]);
    dft4<-1>(q[0].zw, q[1].zw, q[2].zw, q[3].zw, y[1][0], y[1][1], y[1][2], y[1][3]);
#pragma unroll
    for (int k = 0; k < 4; ++k) {
      f32x2 a = y[0][k], b = y[1][k];
      if (k) { a = cmul(a, t4[0][k - 1]); b = cmul(b, t4[1][k - 1]); }
      f32x4 o; o.xy = a; o.zw = b;
      *(f32x4*)(lds + k * PS + 2 * tid) = o;
    }
  }
  SYNC();
  TS(1);
  f32x2* buf = lds + wave * PS;
  f32x2 v[8];
#pragma unroll
  for (int j = 0; j < 8; ++j) v[j] = buf[lane + 64 * j];
  fft512_fwd(v, buf, tw, lane);
  TS(2);
  if (kd != 0) {
#pragma unroll
    for (int k2 = 0; k2 < 8; ++k2) v[k2] = cmul(v[k2], unpk(wv[k2]));
  } else {
    f32x4* ex = (f32x4*)(lds + 4 * PS);
#pragma unroll
    for (int k2 = 0; k2 < 8; ++k2) {
      f32x4 e; e.xy = v[k2]; e.z = U2F(wv[k2]); e.w = U2F(wx[k2]);
      ex[kbase + 256 * k2] = e;
    }
    SYNC();
#pragma unroll
    for (int k2 = 0; k2 < 8; ++k2) {
      const int k = kbase + 256 * k2, kn = (2048 - k) & 2047;
      const f32x4 e = ex[kn];
      const f32x2 c1 = v[k2], c2 = e.xy;
      f32x2 r0, r1;
      r0.x = 0.5f * (c1.x + c2.x); r0.y = 0.5f * (c1.y - c2.y);
      r1.x = 0.5f * (c1.y + c2.y); r1.y = 0.5f * (c2.x - c1.x);
      const u32x4 eu = __builtin_bit_cast(u32x4, e);
      const u32 ez = eu.z, ew = eu.w;
      const f32x2 a1 = unpk(wv[k2]), a2 = unpk(ez), b1 = unpk(wx[k2]), b2 = unpk(ew);
      f32x2 h0, h1;
      h0.x = 0.5f * (a1.x + a2.x); h0.y = 0.5f * (a1.y - a2.y);
      h1.x = 0.5f * (b1.x + b2.x); h1.y = 0.5f * (b1.y - b2.y);
      const f32x2 p0 = cmul(r0, h0), p1 = cmul(r1, h1);
      v[k2].x = p0.x - p1.y;
      v[k2].y = p0.y + p1.x;
    }
  }
  fft512_inv(v, buf, tw, lane);
  TS(3);
  wave_sync();
#pragma unroll
  for (int j = 0; j < 8; ++j) buf[lane + 64 * j] = v[j];
  SYNC();
  {
    f32x2 y[2][4];
#pragma unroll
    for (int k = 0; k < 4; ++k) {
      const f32x4 o = *(const f32x4*)(lds + k * PS + 2 * tid);
      y[0][k] = o.xy; y[1][k] = o.zw;
      if (k) { y[0][k] = cmulc(y[0][k], t4[0][k - 1]); y[1][k] = cmulc(y[1][k], t4[1][k - 1]); }
    }
    f32x2 o[2][4];
    dft4<1>(y[0][0], y[0][1], y[0][2], y[0][3], o[0][0], o[0][1], o[0][2], o[0][3]);
    dft4<1>(y[1][0], y[1][1], y[1][2], y[1][3], o[1][0], o[1][1], o[1][2], o[1][3]);
    const rsrc_t rw2 = mkrsrc(ws);
#pragma unroll
    for (int n3 = 0; n3 < 4; ++n3) {
      f32x4 w; w.xy = o[0][n3]; w.zw = o[1][n3];
      st16(rw2, (u32)WS_ZT + (u32)(kd * SEQ * 8 + (tid + 256 * n3) * 16), __builtin_bit_cast(u32x4, w));
    }
  }
  WAIT_V(0);
  SYNC();
  u32* sync = (u32*)(ws + WS_SYNC);
  if (tid == 0) {
    if (__hip_atomic_fetch_add(sync + (kd & 7) * 16, 1u, __ATOMIC_RELAXED, __HIP_MEMORY_SCOPE_AGENT) == 31)
      __hip_atomic_fetch_add(sync + 40 * 16, 1u, __ATOMIC_RELAXED, __HIP_MEMORY_SCOPE_AGENT);
  }
  TS(4);

  const int s0 = kd * 8;
  const int row = s0 + 2 * wave;
  const float g0 = ((const float*)(ws + WS_G))[row], g1 = ((const float*)(ws + WS_G))[row + 1];
  if (wave == 0) {
    for (int it = 0; it < (1 << 22); ++it) {
      if (__hip_atomic_load(sync + 40 * 16, __ATOMIC_RELAXED, __HIP_MEMORY_SCOPE_AGENT) >= 8) break;
      __builtin_amdgcn_s_sleep(4);
    }
  }
  SYNC();
  TS(5);
  const rsrc_t rws = mkrsrc(ws);
  u32x4 z[4];
#pragma unroll
  for (int r = 0; r < 4; ++r) {
    const int i = tid + 256 * r;
    z[r] = __builtin_amdgcn_raw_buffer_load_b128(rws, (u32)WS_ZT + (u32)((i >> 2) * SEQ + s0 + 2 * (i & 3)) * 8, 0, 16);
  }
#pragma unroll
  for (int r = 0; r < 4; ++r) {
    const int i = tid + 256 * r;
    const int kk = i >> 2, p = i & 3;
    const f32x4 qq = __builtin_bit_cast(f32x4, z[r]);
    f32x2 c1, c2;
    if (kk) {
      c1.x = qq.x - qq.w; c1.y = qq.y + qq.z;
      c2.x = qq.x + qq.w; c2.y = qq.z - qq.y;
    } else {
      c1.x = qq.x; c1.y = qq.z;
      c2.x = qq.y; c2.y = qq.w;
    }
    lds[p * PS3 + sslot(kk)] = c1;
    lds[p * PS3 + sslot(kk ? 512 - kk : 256)] = c2;
  }
  SYNC();
  TS(6);
  f32x2* buf3 = lds + wave * PS3;
#pragma unroll
  for (int k2 = 0; k2 < 8; ++k2) v[k2] = buf3[sslot(hi + 8 * lo + 64 * k2)];
  fft512_inv(v, buf3, tw, lane);
  wave_sync();
#pragma unroll
  for (int j = 0; j < 8; ++j) buf3[lane + 2 * (lane >> 3) + 80 * j] = v[j];
  wave_sync();
  f32x4 u[4];
#pragma unroll
  for (int j = 0; j < 4; ++j) u[j] = *(const f32x4*)(buf3 + 10 * lane + 2 * j);
  TS(7);
  for (int it = 0; it < (1 << 22); ++it) {
    if (__hip_atomic_load(sync + (8 + (s0 >> 6)) * 16, __ATOMIC_RELAXED, __HIP_MEMORY_SCOPE_AGENT) >= 8) break;
    __builtin_amdgcn_s_sleep(2);
  }
  asm volatile("" ::: "memory");
  TS(8);
  const u32x4 b0 = __builtin_amdgcn_raw_buffer_load_b128(rws, (u32)WS_B + (u32)(row * DM + lane * 8) * 2, 0, 16);
  const u32x4 b1 = __builtin_amdgcn_raw_buffer_load_b128(rws, (u32)WS_B + (u32)((row + 1) * DM + lane * 8) * 2, 0, 16);
  const float sc = 1.0f / (2048.0f * 512.0f);
  const float og0 = bfr(1.0f - g0), og1 = bfr(1.0f - g1);
  u32x4 o0, o1;
#pragma unroll
  for (int j = 0; j < 4; ++j) {
    const f32x2 bv0 = unpk(b0[j]), bv1 = unpk(b1[j]);
    o0[j] = pack2(bfr(g0 * bfr(u[j].x * sc)) + bfr(og0 * bv0.x), bfr(g0 * bfr(u[j].z * sc)) + bfr(og0 * bv0.y));
    o1[j] = pack2(bfr(g1 * bfr(u[j].y * sc)) + bfr(og1 * bv1.x), bfr(g1 * bfr(u[j].w * sc)) + bfr(og1 * bv1.y));
  }
  const rsrc_t ro = mkrsrc(out);
  st16(ro, (u32)(row * DM + lane * 8) * 2, o0);
  st16(ro, (u32)((row + 1) * DM + lane * 8) * 2, o1);
  TS(9);
}

NOPK __global__ __launch_bounds__(256, 2) void k_l2(char* __restrict__ ws, u16* __restrict__ out) {
  extern __shared__ __attribute__((aligned(16))) char smem[];
  if (BIDX >= 504) {
    const u32 d = code_prefetch(TIDX, 224);
    WAIT_V(0);
    asm volatile("" ::"v"(d));
  }
  if (BIDX >= 256) g2_body(ws, smem, BIDX - 256);
  else k2_body(ws, smem, BIDX, out);
}

extern "C" void kernel_launch(void* const* d_in, const int* in_sizes, int n_in, void* d_out, int out_size,
                              void* d_ws, size_t ws_size, hipStream_t stream) {
  const u16* x = (const u16*)d_in[0];
  const u16* wr = (const u16*)d_in[1];
  const u16* wi = (const u16*)d_in[2];
  const u16* wg = (const u16*)d_in[4];
  const u16* wu = (const u16*)d_in[5];
  const u16* wd = (const u16*)d_in[6];
  const u16* gw = (const u16*)d_in[7];
  char* ws = (char*)d_ws;
  (void)hipFuncSetAttribute((const void*)k_l1, hipFuncAttributeMaxDynamicSharedMemorySize, L1_LDS);
  (void)hipFuncSetAttribute((const void*)k_l2, hipFuncAttributeMaxDynamicSharedMemorySize, L2_LDS);
  k_l1<<<256, 1024, L1_LDS, stream>>>(x, wr, wi, wg, wu, wd, gw, ws);
  k_l2<<<512, 256, L2_LDS, stream>>>(ws, (u16*)d_out);
}
```

```cpp
#include <hip/hip_runtime.h>
#include <hip/hip_bf16.h>
#include <stdint.h>

#if defined(__HIP_DEVICE_COMPILE__)
#define NOPK __attribute__((target("no-packed-fp32-ops")))
#else
#define NOPK
#endif
#define DEVI NOPK __device__ __forceinline__
#define TS(i)

typedef __attribute__((ext_vector_type(8))) short bf16x8;
typedef __attribute__((ext_vector_type(4))) short bf16x4;
typedef __attribute__((ext_vector_type(4))) float f32x4;
typedef __attribute__((ext_vector_type(2))) float f32x2;
typedef unsigned short u16;
typedef unsigned int u32;
typedef __attribute__((ext_vector_type(4))) unsigned int u32x4;
typedef __attribute__((ext_vector_type(2))) unsigned int u32x2;
typedef __attribute__((address_space(3))) bf16x4* lds_b4_ptr;
#define TIDX ((int)__builtin_amdgcn_workitem_id_x())
#define BIDX ((int)__builtin_amdgcn_workgroup_id_x())
#define U2F(x) __builtin_bit_cast(float, (u32)(x))
#define SYNC() do { __builtin_amdgcn_fence(__ATOMIC_RELEASE, "workgroup"); __builtin_amdgcn_s_barrier(); \
                    __builtin_amdgcn_fence(__ATOMIC_ACQUIRE, "workgroup"); } while (0)
DEVI float fexp(float x) { return __builtin_amdgcn_exp2f(x * 1.44269504088896340736f); }
DEVI float shfl_xor(float v, int m, int lane) {
  return __builtin_bit_cast(float, __builtin_amdgcn_ds_bpermute((lane ^ m) << 2, __builtin_bit_cast(int, v)));
}

constexpr int SEQ = 2048, DM = 512, DF = 257, FF = 1376;
constexpr int KS32 = FF / 32;

constexpr size_t WS_YT = 0;
constexpr size_t WS_WT = WS_YT + (size_t)DF * SEQ * 8;
constexpr size_t WS_H = WS_WT + (size_t)DF * SEQ * 4;
constexpr size_t WS_WD = WS_H + (size_t)SEQ * FF * 2;
constexpr size_t WS_B = WS_WD + (size_t)FF * DM * 2;
constexpr size_t WS_G = WS_B + (size_t)SEQ * DM * 2;
constexpr size_t WS_SYNC = WS_G + 8192;
constexpr size_t WS_ZT = WS_SYNC + 4096;

DEVI float bf2f(u16 v) { return U2F(((u32)v) << 16); }
DEVI u16 f2bf(float f) {
  __bf16 b = (__bf16)f;
  return __builtin_bit_cast(u16, b);
}
DEVI float bfr(float f) { return bf2f(f2bf(f)); }

DEVI f32x2 cmul(f32x2 a, f32x2 b) {
  f32x2 r;
  r.x = a.x * b.x - a.y * b.y;
  r.y = a.x * b.y + a.y * b.x;
  return r;
}
DEVI f32x2 cmulc(f32x2 a, f32x2 b) {
  f32x2 r;
  r.x = a.x * b.x + a.y * b.y;
  r.y = a.y * b.x - a.x * b.y;
  return r;
}
DEVI f32x2 twid(float frac) {
  f32x2 r;
  r.x = __builtin_amdgcn_cosf(frac);
  r.y = -__builtin_amdgcn_sinf(frac);
  return r;
}

template <int SGN>
DEVI f32x2 mul_i(f32x2 z) {
  f32x2 r;
  if (SGN < 0) { r.x = z.y; r.y = -z.x; } else { r.x = -z.y; r.y = z.x; }
  return r;
}
template <int SGN>
DEVI void dft4(f32x2 b0, f32x2 b1, f32x2 b2, f32x2 b3, f32x2& y0, f32x2& y1, f32x2& y2,
                                     f32x2& y3) {
  f32x2 c0 = b0 + b2, c1 = b0 - b2, c2 = b1 + b3, c3 = mul_i<SGN>(b1 - b3);
  y0 = c0 + c2; y2 = c0 - c2; y1 = c1 + c3; y3 = c1 - c3;
}
template <int SGN>
DEVI void dft8(f32x2 (&v)[8]) {
  const float R = 0.70710678118654752440f;
  f32x2 a0 = v[0] + v[4], a4 = v[0] - v[4];
  f32x2 a1 = v[1] + v[5], a5 = v[1] - v[5];
  f32x2 a2 = v[2] + v[6], a6 = v[2] - v[6];
  f32x2 a3 = v[3] + v[7], a7 = v[3] - v[7];
  f32x2 t5, t7;
  if (SGN < 0) {
    t5.x = (a5.x + a5.y) * R; t5.y = (a5.y - a5.x) * R;
    t7.x = (a7.y - a7.x) * R; t7.y = -(a7.x + a7.y) * R;
  } else {
    t5.x = (a5.x - a5.y) * R; t5.y = (a5.x + a5.y) * R;
    t7.x = -(a7.x + a7.y) * R; t7.y = (a7.x - a7.y) * R;
  }
  f32x2 t6 = mul_i<SGN>(a6);
  dft4<SGN>(a0, a1, a2, a3, v[0], v[2], v[4], v[6]);
  dft4<SGN>(a4, t5, t6, t7, v[1], v[3], v[5], v[7]);
}

DEVI void wave_sync() {
  asm volatile("s_waitcnt lgkmcnt(0)" ::: "memory");
  __builtin_amdgcn_wave_barrier();
}

struct Tw512 {
  f32x2 a[7];
  f32x2 b[7];
};
DEVI void make_tw512(Tw512& t, int lane) {
#pragma unroll
  for (int k = 1; k < 8; ++k) {
    t.a[k - 1] = twid((float)((lane * k) & 511) * (1.0f / 512.0f));
    t.b[k - 1] = twid((float)(((lane & 7) * k) & 63) * (1.0f / 64.0f));
  }

}

DEVI void fft512_fwd(f32x2 (&v)[8], f32x2* buf, const Tw512& tw, int lane) {
  const int hi = lane >> 3, lo = lane & 7;
  dft8<-1>(v);
#pragma unroll
  for (int k = 1; k < 8; ++k) v[k] = cmul(v[k], tw.a[k - 1]);
  wave_sync();
#pragma unroll
  for (int k = 0; k < 8; ++k) buf[72 * k + lane] = v[k];
  wave_sync();
#pragma unroll
  for (int n = 0; n < 8; ++n) v[n] = buf[72 * hi + 8 * n + lo];
  dft8<-1>(v);
#pragma unroll
  for (int k = 1; k < 8; ++k) v[k] = cmul(v[k], tw.b[k - 1]);
  wave_sync();
#pragma unroll
  for (int k = 0; k < 8; ++k) buf[72 * hi + 9 * k + lo] = v[k];
  wave_sync();
#pragma unroll
  for (int n = 0; n < 8; ++n) v[n] = buf[72 * hi + 9 * lo + n];
  dft8<-1>(v);
}
DEVI void fft512_inv(f32x2 (&v)[8], f32x2* buf, const Tw512& tw, int lane) {
  const int hi = lane >> 3, lo = lane & 7;
  dft8<1>(v);
  wave_sync();
#pragma unroll
  for (int n = 0; n < 8; ++n) buf[72 * hi + 9 * lo + n] = v[n];
  wave_sync();
#pragma unroll
  for (int k = 0; k < 8; ++k) v[k] = buf[72 * hi + 9 * k + lo];
#pragma unroll
  for (int k = 1; k < 8; ++k) v[k] = cmulc(v[k], tw.b[k - 1]);
  dft8<1>(v);
  wave_sync();
#pragma unroll
  for (int n = 0; n < 8; ++n) buf[72 * hi + 8 * n + lo] = v[n];
  wave_sync();
#pragma unroll
  for (int k = 0; k < 8; ++k) v[k] = buf[72 * k + lane];
#pragma unroll
  for (int k = 1; k < 8; ++k) v[k] = cmulc(v[k], tw.a[k - 1]);
  dft8<1>(v);
}

DEVI int sslot(int k) { return k + 4 * (k >> 5); }
constexpr int PS = 584;

#define PREFETCH 0
#define AUX_GATE 7
constexpr int G1_NS = 3;
constexpr int G1_X_OFF = G1_NS * 24576;
constexpr int AX_OFF = G1_X_OFF + G1_NS * 16384;
constexpr int AX_W_OFF = AX_OFF + 4 * PS * 8;
constexpr int AX_WD_OFF = AX_W_OFF + 10240;
constexpr int AX_GW_OFF = AX_WD_OFF;
constexpr int L1_CNT_OFF = AX_WD_OFF + 8192;
constexpr int L1_LDS = L1_CNT_OFF + 256;

#define WAIT_V(n) asm volatile("s_waitcnt vmcnt(" #n ")" ::: "memory")
#define WAIT_L(n) asm volatile("s_waitcnt lgkmcnt(" #n ")" ::: "memory")
#define RAW_BAR() asm volatile("s_barrier" ::: "memory")

typedef __amdgpu_buffer_rsrc_t rsrc_t;
typedef __attribute__((ext_vector_type(4))) int i32x4;
DEVI void dma16(const void* gsrc, u32 lds_dst) {
  u32 keep;
  asm volatile("s_mov_b32 %0, m0\n\ts_mov_b32 m0, %2\n\ts_nop 0\n\tglobal_load_lds_dwordx4 %1, off\n\ts_mov_b32 m0, %0"
               : "=&s"(keep) : "v"(gsrc), "s"(lds_dst) : "memory");
}
DEVI void dma16b(i32x4 rs, u32 voff, u32 soff, u32 lds_dst) {
  u32 keep;
  asm volatile("s_mov_b32 %0, m0\n\ts_mov_b32 m0, %4\n\ts_nop 0\n\tbuffer_load_dwordx4 %1, %2, %3 offen lds\n\ts_mov_b32 m0, %0"
               : "=&s"(keep) : "v"(voff), "s"(rs), "s"(soff), "s"(lds_dst) : "memory");
}
DEVI i32x4 mkrs(const void* p, u32 bytes) {
  const unsigned long long a = (unsigned long long)p;
  i32x4 r;
  r.x = __builtin_amdgcn_readfirstlane((int)(u32)a);
  r.y = __builtin_amdgcn_readfirstlane((int)(u32)(a >> 32) & 0xffff);
  r.z = (int)bytes;
  r.w = 0x00020000;
  return r;
}
DEVI rsrc_t mkrsrc(const void* p) { return __builtin_amdgcn_make_buffer_rsrc((void*)p, 0, 0x7fffffff, 0x00020000); }
DEVI void st16(rsrc_t r, u32 off, u32x4 v) { __builtin_amdgcn_raw_buffer_store_b128(v, r, off, 0, 16); }
DEVI u32 pack2(float a, float b) { return (u32)f2bf(a) | ((u32)f2bf(b) << 16); }

typedef __attribute__((address_space(3))) u32* lds_u32_ptr;
DEVI void lds_signal(u32 addr, int lane) {
  unsigned long long keep;
  asm volatile("s_mov_b64 %0, exec\n\ts_mov_b64 exec, 1\n\tds_add_u32 %1, %2\n\ts_mov_b64 exec, %0"
               : "=&s"(keep) : "v"(addr), "v"(1u) : "memory");
}
DEVI void lds_wait1(u32 addr, u32 need) {
  while (true) {
    u32 v;
    asm volatile("ds_read_b32 %0, %1\n\ts_waitcnt lgkmcnt(0)" : "=v"(v) : "v"(addr) : "memory");
    if ((u32)__builtin_amdgcn_readfirstlane(v) >= need) break;
    __builtin_amdgcn_s_sleep(1);
  }
}
DEVI void lds_wait2(u32 addr, u32 need0, u32 need1) {
  while (true) {
    u32x2 v;
    asm volatile("ds_read_b64 %0, %1\n\ts_waitcnt lgkmcnt(0)" : "=v"(v) : "v"(addr) : "memory");
    const u32 a = __builtin_amdgcn_readfirstlane(v.x), b = __builtin_amdgcn_readfirstlane(v.y);
    if (a >= need0 && b >= need1) break;
    __builtin_amdgcn_s_sleep(1);
  }
}

DEVI void aux_body(const u16* __restrict__ x, const u16* __restrict__ wr, const u16* __restrict__ wi,
                   const u16* __restrict__ wd, const u16* __restrict__ gw, char* __restrict__ ws, char* smem, int a,
                   int wave, int lane, bool gated) {
  constexpr int TSK = 0;
  const u32 lds0 = (u32)(size_t)(__attribute__((address_space(3))) char*)smem;
  const rsrc_t rws = mkrsrc(ws);
  const int s0 = a * 8;
  const int tid = wave * 64 + lane;
  f32x2* aux = (f32x2*)(smem + AX_OFF);
  {
    const int p = wave;
    Tw512 tw;
    make_tw512(tw, lane);
    WAIT_V(0);
    TS(1);
    const u16* xa = (const u16*)(aux + p * PS);
    const u16* gl = (const u16*)(smem + AX_GW_OFF + p * 1024);
    u16 ra[8], rb[8], rg[8];
#pragma unroll
    for (int j = 0; j < 8; ++j) {
      ra[j] = xa[lane + 64 * j];
      rb[j] = xa[DM + lane + 64 * j];
      rg[j] = gl[lane + 64 * j];
    }
    f32x2 v[8];
    float sa = 0.f, sb = 0.f;
#pragma unroll
    for (int j = 0; j < 8; ++j) {
      v[j].x = bf2f(ra[j]);
      v[j].y = bf2f(rb[j]);
      float w = bf2f(rg[j]);
      sa += bfr(v[j].x * w);
      sb += bfr(v[j].y * w);
    }
#pragma unroll
    for (int o = 32; o > 0; o >>= 1) {
      sa += shfl_xor(sa, o, lane);
      sb += shfl_xor(sb, o, lane);
    }
    if (lane < 2) {
      float s = bfr(lane ? sb : sa);
      float gg = bfr(1.0f / (1.0f + fexp(-s)));
      ((float*)(ws + WS_G))[s0 + 2 * p + lane] = gg;
    }
    f32x2* buf = aux + p * PS;
    fft512_fwd(v, buf, tw, lane);
    wave_sync();
    const int hi = lane >> 3, lo = lane & 7;
#pragma unroll
    for (int k2 = 0; k2 < 8; ++k2) buf[sslot(hi + 8 * lo + 64 * k2)] = v[k2];
  }
  WAIT_L(0);
  lds_signal(lds0 + L1_CNT_OFF + 42 * 4, lane);
  TS(2);
  if (gated) lds_wait1(lds0 + L1_CNT_OFF + 44 * 4, 4);
  {
    const int p = wave;
    const char* pr = (const char*)wr + (size_t)a * 4112;
    const char* pi = (const char*)wi + (size_t)a * 4112;
    dma16(pr + p * 1024 + lane * 16, lds0 + AX_W_OFF + p * 1024);
    dma16(pi + p * 1024 + lane * 16, lds0 + AX_W_OFF + 5120 + p * 1024);
    if (p < 2) dma16((p ? pi : pr) + (lane == 0 ? 4096 : 0), lds0 + AX_W_OFF + p * 5120 + 4096);
    const char* pd = (const char*)wd + (size_t)(a < 172 ? a : 0) * 8192;
    dma16(pd + p * 1024 + lane * 16, lds0 + AX_WD_OFF + p * 1024);
    dma16(pd + 4096 + p * 1024 + lane * 16, lds0 + AX_WD_OFF + 4096 + p * 1024);
  }
  lds_wait1(lds0 + L1_CNT_OFF + 42 * 4, 4);
#pragma unroll
  for (int r = 0; r < 4; ++r) {
    const int i = tid + 256 * r;
    const int kd = i >> 2, p = i & 3;
    const f32x2 z1 = aux[p * PS + sslot(kd)];
    const f32x2 z2 = aux[p * PS + sslot(kd ? 512 - kd : 256)];
    f32x4 o;
    if (kd) {
      o.x = 0.5f * (z1.x + z2.x);
      o.y = 0.5f * (z1.y - z2.y);
      o.z = 0.5f * (z1.y + z2.y);
      o.w = 0.5f * (z2.x - z1.x);
    } else {
      o.x = z1.x; o.y = z2.x; o.z = z1.y; o.w = z2.y;
    }
    st16(rws, (u32)WS_YT + (u32)(kd * SEQ + s0 + 2 * p) * 8, __builtin_bit_cast(u32x4, o));
  }
  TS(3);
  WAIT_V(4);
  lds_signal(lds0 + L1_CNT_OFF + 43 * 4, lane);
  lds_wait1(lds0 + L1_CNT_OFF + 43 * 4, 4);
  {
    const u16* lr = (const u16*)(smem + AX_W_OFF);
    const u16* li = (const u16*)(smem + AX_W_OFF + 5120);
#pragma unroll
    for (int r = 0; r < 3; ++r) {
      const int i = tid + 256 * r;
      if (i < DF * 2) {
        const int kd = i >> 1, h = i & 1;
        u32x4 o;
#pragma unroll
        for (int q = 0; q < 4; ++q) o[q] = (u32)lr[(h * 4 + q) * DF + kd] | ((u32)li[(h * 4 + q) * DF + kd] << 16);
        st16(rws, (u32)WS_WT + (u32)(kd * SEQ + s0 + h * 4) * 4, o);
      }
    }
  }
  if (a < 172) {
    const u16* ld = (const u16*)(smem + AX_WD_OFF);
#pragma unroll
    for (int r = 0; r < 2; ++r) {
      const int n = tid + 256 * r;
      u32x4 o;
#pragma unroll
      for (int q = 0; q < 4; ++q) o[q] = (u32)ld[(2 * q) * DM + n] | ((u32)ld[(2 * q + 1) * DM + n] << 16);
      const int ks = a >> 2, kq = a & 3;
      st16(rws, (u32)WS_WD + (u32)(((ks * 32 + (n >> 4)) * 64 + (n & 15) + 16 * kq) * 16), o);
    }
  }
  TS(4);
}

NOPK __global__ __launch_bounds__(1024, 4) void k_l1(const u16* __restrict__ x, const u16* __restrict__ wr,
                                                const u16* __restrict__ wi, const u16* __restrict__ wg,
                                                const u16* __restrict__ wu, const u16* __restrict__ wd,
                                                const u16* __restrict__ gw, char* __restrict__ ws) {
  extern __shared__ __attribute__((aligned(16))) char smem[];
  const int bid = BIDX;
  const int tid = TIDX, lane = tid & 63;
  const int wave = __builtin_amdgcn_readfirstlane(tid >> 6);
  constexpr int TSK = 0;
  TS(0);
  const u32 lds0 = (u32)(size_t)(__attribute__((address_space(3))) char*)smem;
  const u32 cnt = lds0 + L1_CNT_OFF;
  if (wave == 0) { ((u32*)(smem + L1_CNT_OFF))[lane] = 0; WAIT_L(0); }
  RAW_BAR();
  const int xcd = bid & 7, idx = bid >> 3;
  const int T = xcd * 30 + (idx < 30 ? idx : 0);
  const int fg = T / 80, rem = T - fg * 80;
  const int mt_ = rem / 5, fi = rem - mt_ * 5;
  const int m0 = mt_ * 128, f0 = (fg * 5 + fi) * 96;
  if (wave >= 12) {
    __builtin_amdgcn_s_setprio(2);
    const int w = wave - 12;
    if (bid == 255 && w == 0) ((u32*)(ws + WS_SYNC))[lane * 16] = 0;
    const char* px = (const char*)x + (size_t)(bid * 8 + 2 * w) * 1024;
    dma16(px + lane * 16, lds0 + AX_OFF + w * (PS * 8));
    dma16(px + 1024 + lane * 16, lds0 + AX_OFF + w * (PS * 8) + 1024);
    dma16((const char*)gw + lane * 16, lds0 + AX_GW_OFF + w * 1024);
    aux_body(x, wr, wi, wd, gw, ws, smem, bid, w, lane, idx < 30);
    return;
  }
  if (idx >= 30) return;

  {
    const int q4 = wave >= 8 ? wave - 8 : (wave >> 1);
    const i32x4 rx = mkrs(x, SEQ * DM * 2), rg = mkrs(wg, DM * FF * 2), ru = mkrs(wu, DM * FF * 2);
    u32 vx, vw[3];
    {
      const int row = lane >> 3;
      const int c = (lane & 7) ^ row;
      vx = (u32)((m0 + q4 * 32 + row) * DM + c * 8) * 2;
#pragma unroll
      for (int j = 0; j < 3; ++j) {
        const int q = 64 * j + lane;
        const int k = q / 12, cp = q - k * 12;
        const int c2 = cp ^ (((k >> 3) & 1) << 1);
        vw[j] = (u32)((q4 * 16 + k) * FF + f0 + c2 * 8) * 2;
      }
    }
    auto issue = [&](int s, int lo, int hi) NOPK __attribute__((always_inline)) {
      const int slot = s % G1_NS;
      const u32 wdst = lds0 + slot * 24576 + q4 * 3072, xdst = lds0 + G1_X_OFF + slot * 16384 + q4 * 4096;
      const u32 sw = s * (64 * FF * 2);
#pragma unroll
      for (int n = 0; n < 10; ++n) {
        if (n >= lo && n < hi) {
          if (n < 3) dma16b(rg, vw[n], sw, wdst + n * 1024);
          else if (n < 7) dma16b(rx, vx, s * 128 + (n - 3) * 8192, xdst + (n - 3) * 1024);
          else dma16b(ru, vw[n - 7], sw, wdst + 12288 + (n - 7) * 1024);
        }
      }
    };
    if (wave >= 8) {
      issue(1, 0, 10);
      issue(2, 0, 5);
      WAIT_V(5);
      lds_signal(cnt + 1 * 4, lane);
      issue(2, 5, 10);
#pragma unroll 1
      for (int s = 3; s < 8; ++s) {
        lds_wait1(cnt + (16 + s - 3) * 4, 8);
        issue(s, 0, 3);
        WAIT_V(3);
        lds_signal(cnt + (s - 1) * 4, lane);
        issue(s, 3, 10);
        if (s == AUX_GATE) lds_signal(cnt + 44 * 4, lane);
      }
      WAIT_V(0);
      lds_signal(cnt + 7 * 4, lane);
      return;
    }
    if (wave & 1) issue(0, 5, 10); else issue(0, 0, 5);
    WAIT_V(0);
    lds_signal(cnt, lane);
  }

  const int wm = wave >> 1, wf = wave & 1;
  const int l15 = lane & 15, lq = lane >> 4;
  int xoff[2][2];
#pragma unroll
  for (int mt = 0; mt < 2; ++mt)
#pragma unroll
    for (int kk = 0; kk < 2; ++kk) {
      const int row = wm * 32 + mt * 16 + l15;
      xoff[mt][kk] = G1_X_OFF + row * 128 + (((kk * 4 + lq) ^ (row & 7)) << 4);
    }
  const int wq = l15 >> 2, wp = l15 & 3;
  int woff[3];
#pragma unroll
  for (int ft = 0; ft < 3; ++ft)
    woff[ft] = (8 * lq + wq) * 192 + (((2 * (wf * 3 + ft) + (wp >> 1)) ^ (2 * (lq & 1))) << 4) + (wp & 1) * 8;

  f32x4 accg[3][2], accu[3][2];
#pragma unroll
  for (int a = 0; a < 3; ++a)
#pragma unroll
    for (int b = 0; b < 2; ++b) { accg[a][b] = (f32x4)0.f; accu[a][b] = (f32x4)0.f; }

  bf16x8 xa[2], xb[2], wgf[3], wuf[3];
  auto rdx = [&](bf16x8(&xf)[2], int xs, int kk) NOPK __attribute__((always_inline)) {
#pragma unroll
    for (int mt = 0; mt < 2; ++mt) xf[mt] = *(const bf16x8*)(smem + xs + xoff[mt][kk]);
  };
  auto rdw = [&](bf16x8(&wfr)[3], int wsl, int kk, int mat) NOPK __attribute__((always_inline)) {
#pragma unroll
    for (int n = 0; n < 3; ++n) {
      const char* base = smem + (wsl + woff[n]) + kk * 6144 + mat * 12288;
      bf16x4 lo = __builtin_amdgcn_ds_read_tr16_b64_v4i16((lds_b4_ptr)(base));
      bf16x4 hi = __builtin_amdgcn_ds_read_tr16_b64_v4i16((lds_b4_ptr)(base + 768));
      wfr[n] = __builtin_shufflevector(lo, hi, 0, 1, 2, 3, 4, 5, 6, 7);
    }
  };
  auto mma = [&](f32x4(&acc)[3][2], bf16x8(&xf)[2], bf16x8(&wfr)[3]) NOPK __attribute__((always_inline)) {
#pragma unroll
    for (int ft = 0; ft < 3; ++ft)
#pragma unroll
      for (int mt = 0; mt < 2; ++mt)
        acc[ft][mt] = __builtin_amdgcn_mfma_f32_16x16x32_bf16(wfr[ft], xf[mt], acc[ft][mt], 0, 0, 0);
  };
  auto interleave = [&]() NOPK __attribute__((always_inline)) {
#pragma unroll
    for (int i = 0; i < 6; ++i) {
      __builtin_amdgcn_sched_group_barrier(0x008, 1, 0);
      __builtin_amdgcn_sched_group_barrier(0x100, 2, 0);
    }
  };
  const volatile u32* cv = (const volatile u32*)(smem + L1_CNT_OFF);

  TS(1);
  lds_wait1(cnt, 8);
  TS(4);
  rdx(xa, 0, 0);
  rdw(wgf, 0, 0, 0);
#define SB() __builtin_amdgcn_sched_barrier(0)
  int slot = 0;
#pragma unroll 1
  for (int S = 0; S < 8; ++S) {
    const int wsl = slot * 24576, xs = slot * 16384;
    slot = slot == G1_NS - 1 ? 0 : slot + 1;
    const int wsn = slot * 24576, xn = slot * 16384;
    SB(); rdw(wuf, wsl, 0, 1); mma(accg, xa, wgf); interleave();
    SB(); rdw(wgf, wsl, 1, 0); rdx(xb, xs, 1); mma(accu, xa, wuf); interleave();
    SB(); rdw(wuf, wsl, 1, 1); const u32 fv = cv[S + 1]; mma(accg, xb, wgf); interleave();
    SB();
    WAIT_L(0);
    lds_signal(cnt + (16 + S) * 4, lane);
    if (S < 7) {
      if ((u32)__builtin_amdgcn_readfirstlane(fv) < 4) lds_wait1(cnt + (S + 1) * 4, 4);
    }
    SB();
    rdw(wgf, wsn, 0, 0); rdx(xa, xn, 0);
    mma(accu, xb, wuf);
    interleave();
  }
  SB();
  TS(5);
  const rsrc_t rws = mkrsrc(ws);
  const int lane2 = __builtin_amdgcn_mbcnt_hi(~0u, __builtin_amdgcn_mbcnt_lo(~0u, 0u));
  const int e15 = lane2 & 15, eq = lane2 >> 4;
#pragma unroll
  for (int ft = 0; ft < 3; ++ft) {
    u32 pk[2][2];
#pragma unroll
    for (int mt = 0; mt < 2; ++mt) {
      float o[4];
#pragma unroll
      for (int r = 0; r < 4; ++r) {
        const float g = bfr(accg[ft][mt][r]);
        const float sl = bfr(g * __builtin_amdgcn_rcpf(1.0f + fexp(-g)));
        const float u = bfr(accu[ft][mt][r]);
        o[r] = sl * u;
      }
      pk[mt][0] = pack2(o[0], o[1]);
      pk[mt][1] = pack2(o[2], o[3]);
    }
    auto s0_ = __builtin_amdgcn_permlane16_swap(pk[0][0], pk[1][0], false, false);
    auto s1_ = __builtin_amdgcn_permlane16_swap(pk[0][1], pk[1][1], false, false);
    u32x4 ov;
    ov.x = s0_[0]; ov.y = s1_[0]; ov.z = s0_[1]; ov.w = s1_[1];
    const int f = f0 + (wf * 3 + ft) * 16 + 8 * (eq >> 1);
    const int m = m0 + wm * 32 + (eq & 1) * 16 + e15;
    if (f < FF)
      st16(rws, (u32)WS_H + (u32)(((m >> 4) * KS32 + (f >> 5)) * 1024 + ((m & 15) + 16 * ((f & 31) >> 3)) * 16), ov);
  }
  TS(6);
}

constexpr int L2_LDS = 4 * PS * 8 + 32768;
constexpr int G2_DEPTH = 4;

DEVI void g2_body(char* __restrict__ ws, char* smem, int bid) {
  const int tid = TIDX, lane = tid & 63;
  const int wave = __builtin_amdgcn_readfirstlane(tid >> 6);
  constexpr int TSK = 1;
  TS(0);
  const int xcd = bid & 7, idx = bid >> 3;
  const int mt64 = xcd * 4 + (idx >> 3), nt64 = idx & 7;
  const char* hb = ws + WS_H + (size_t)(mt64 * 4) * KS32 * 1024 + lane * 16;
  const char* wb = ws + WS_WD + (size_t)(nt64 * 4) * 1024 + lane * 16;
  f32x4 acc[4][4];
#pragma unroll
  for (int a = 0; a < 4; ++a)
#pragma unroll
    for (int b = 0; b < 4; ++b) acc[a][b] = (f32x4)0.f;
  constexpr int NS = G2_DEPTH + 1;
  bf16x8 hf[NS][4], wf[NS][4];
  auto ld = [&](int slot, int i) NOPK __attribute__((always_inline)) {
    int ks = wave + 4 * i;
    ks = ks > KS32 - 1 ? KS32 - 1 : ks;
#pragma unroll
    for (int j = 0; j < 4; ++j) {
      hf[slot][j] = *(const bf16x8*)(hb + ((size_t)j * KS32 + ks) * 1024);
      wf[slot][j] = *(const bf16x8*)(wb + ((size_t)ks * 32 + j) * 1024);
    }
  };
#pragma unroll
  for (int i = 0; i < G2_DEPTH; ++i) ld(i, i);
  __builtin_amdgcn_sched_barrier(0);
#pragma unroll
  for (int i = 0; i < 11; ++i) {
    if (i + G2_DEPTH < 11) ld((i + G2_DEPTH) % NS, i + G2_DEPTH);
    __builtin_amdgcn_sched_barrier(0);
    if (i < 10 || wave < 3) {
#pragma unroll
      for (int nt = 0; nt < 4; ++nt)
#pragma unroll
        for (int mt = 0; mt < 4; ++mt)
          acc[nt][mt] = __builtin_amdgcn_mfma_f32_16x16x32_bf16(wf[i % NS][nt], hf[i % NS][mt], acc[nt][mt], 0, 0, 0);
    }
    __builtin_amdgcn_sched_barrier(0);
  }
  TS(1);
  f32x4* red = (f32x4*)smem;
#pragma unroll
  for (int nt = 0; nt < 4; ++nt) {
    if (nt != wave) {
      const int src = wave < nt ? wave : wave - 1;
#pragma unroll
      for (int mt = 0; mt < 4; ++mt) red[((nt * 3 + src) * 4 + mt) * 64 + lane] = acc[nt][mt];
    }
  }
  SYNC();
  f32x4 fin[4];
#pragma unroll
  for (int nt = 0; nt < 4; ++nt)
    if (nt == wave) {
#pragma unroll
      for (int mt = 0; mt < 4; ++mt) fin[mt] = acc[nt][mt];
    }
  const rsrc_t rws = mkrsrc(ws);
  const int l15 = lane & 15, lq = lane >> 4;
#pragma unroll
  for (int mt = 0; mt < 4; ++mt)
#pragma unroll
    for (int s = 0; s < 3; ++s) fin[mt] += red[((wave * 3 + s) * 4 + mt) * 64 + lane];
#pragma unroll
  for (int pr = 0; pr < 2; ++pr) {
    auto s0_ = __builtin_amdgcn_permlane16_swap(pack2(fin[2 * pr][0], fin[2 * pr][1]),
                                                pack2(fin[2 * pr + 1][0], fin[2 * pr + 1][1]), false, false);
    auto s1_ = __builtin_amdgcn_permlane16_swap(pack2(fin[2 * pr][2], fin[2 * pr][3]),
                                                pack2(fin[2 * pr + 1][2], fin[2 * pr + 1][3]), false, false);
    u32x4 ov;
    ov.x = s0_[0]; ov.y = s1_[0]; ov.z = s0_[1]; ov.w = s1_[1];
    const int m = mt64 * 64 + (2 * pr + (lq & 1)) * 16 + l15;
    const int n = nt64 * 64 + wave * 16 + 8 * (lq >> 1);
    st16(rws, (u32)WS_B + (u32)(m * DM + n) * 2, ov);
  }
  WAIT_V(0);
  SYNC();
  if (tid == 0) __hip_atomic_fetch_add((u32*)(ws + WS_SYNC) + (8 + mt64) * 16, 1u, __ATOMIC_RELAXED, __HIP_MEMORY_SCOPE_AGENT);
  TS(2);
}

DEVI f32x2 unpk(u32 w) {
  f32x2 r;
  r.x = U2F(w << 16);
  r.y = U2F(w & 0xffff0000u);
  return r;
}

constexpr int PS3 = 648;
DEVI void k2_body(char* __restrict__ ws, char* smem, int kd, u16* __restrict__ out) {
  const int tid = TIDX, lane = tid & 63;
  const int wave = __builtin_amdgcn_readfirstlane(tid >> 6);
  constexpr int TSK = 1;
  TS(0);
  const f32x4* col = (const f32x4*)(ws + WS_YT + (size_t)kd * SEQ * 8);
  const u32* wt = (const u32*)(ws + WS_WT) + (size_t)kd * SEQ;
  f32x2* lds = (f32x2*)smem;
  f32x4 q[4];
#pragma unroll
  for (int n3 = 0; n3 < 4; ++n3) q[n3] = col[tid + 256 * n3];
  const int hi = lane >> 3, lo = lane & 7;
  const int kbase = wave + 4 * (hi + 8 * lo);
  u32 wv[8];
#pragma unroll
  for (int k2 = 0; k2 < 8; ++k2) wv[k2] = wt[kbase + 256 * k2];
  u32 wx[8];
  if (kd == 0) {
    const u32* wn = (const u32*)(ws + WS_WT) + (size_t)256 * SEQ;
#pragma unroll
    for (int k2 = 0; k2 < 8; ++k2) wx[k2] = wn[kbase + 256 * k2];
  }
  Tw512 tw;
  make_tw512(tw, lane);
  f32x2 t4[2][3];
#pragma unroll
  for (int h = 0; h < 2; ++h)
#pragma unroll
    for (int k = 1; k < 4; ++k) t4[h][k - 1] = twid((float)(((2 * tid + h) * k) & 2047) * (1.0f / 2048.0f));
  {
    f32x2 y[2][4];
    dft4<-1>(q[0].xy, q[1].xy, q[2].xy, q[3].xy, y[0][0], y[0][1], y[0][2], y[0][3]);
    dft4<-1>(q[0].zw, q[1].zw, q[2].zw, q[3].zw, y[1][0], y[1][1], y[1][2], y[1][3]);
#pragma unroll
    for (int k = 0; k < 4; ++k) {
      f32x2 a = y[0][k], b = y[1][k];
      if (k) { a = cmul(a, t4[0][k - 1]); b = cmul(b, t4[1][k - 1]); }
      f32x4 o; o.xy = a; o.zw = b;
      *(f32x4*)(lds + k * PS + 2 * tid) = o;
    }
  }
  SYNC();
  TS(1);
  f32x2* buf = lds + wave * PS;
  f32x2 v[8];
#pragma unroll
  for (int j = 0; j < 8; ++j) v[j] = buf[lane + 64 * j];
  fft512_fwd(v, buf, tw, lane);
  TS(2);
  if (kd != 0) {
#pragma unroll
    for (int k2 = 0; k2 < 8; ++k2) v[k2] = cmul(v[k2], unpk(wv[k2]));
  } else {
    f32x4* ex = (f32x4*)(lds + 4 * PS);
#pragma unroll
    for (int k2 = 0; k2 < 8; ++k2) {
      f32x4 e; e.xy = v[k2]; e.z = U2F(wv[k2]); e.w = U2F(wx[k2]);
      ex[kbase + 256 * k2] = e;
    }
    SYNC();
#pragma unroll
    for (int k2 = 0; k2 < 8; ++k2) {
      const int k = kbase + 256 * k2, kn = (2048 - k) & 2047;
      const f32x4 e = ex[kn];
      const f32x2 c1 = v[k2], c2 = e.xy;
      f32x2 r0, r1;
      r0.x = 0.5f * (c1.x + c2.x); r0.y = 0.5f * (c1.y - c2.y);
      r1.x = 0.5f * (c1.y + c2.y); r1.y = 0.5f * (c2.x - c1.x);
      const u32x4 eu = __builtin_bit_cast(u32x4, e);
      const u32 ez = eu.z, ew = eu.w;
      const f32x2 a1 = unpk(wv[k2]), a2 = unpk(ez), b1 = unpk(wx[k2]), b2 = unpk(ew);
      f32x2 h0, h1;
      h0.x = 0.5f * (a1.x + a2.x); h0.y = 0.5f * (a1.y - a2.y);
      h1.x = 0.5f * (b1.x + b2.x); h1.y = 0.5f * (b1.y - b2.y);
      const f32x2 p0 = cmul(r0, h0), p1 = cmul(r1, h1);
      v[k2].x = p0.x - p1.y;
      v[k2].y = p0.y + p1.x;
    }
  }
  fft512_inv(v, buf, tw, lane);
  TS(3);
  wave_sync();
#pragma unroll
  for (int j = 0; j < 8; ++j) buf[lane + 64 * j] = v[j];
  SYNC();
  {
    f32x2 y[2][4];
#pragma unroll
    for (int k = 0; k < 4; ++k) {
      const f32x4 o = *(const f32x4*)(lds + k * PS + 2 * tid);
      y[0][k] = o.xy; y[1][k] = o.zw;
      if (k) { y[0][k] = cmulc(y[0][k], t4[0][k - 1]); y[1][k] = cmulc(y[1][k], t4[1][k - 1]); }
    }
    f32x2 o[2][4];
    dft4<1>(y[0][0], y[0][1], y[0][2], y[0][3], o[0][0], o[0][1], o[0][2], o[0][3]);
    dft4<1>(y[1][0], y[1][1], y[1][2], y[1][3], o[1][0], o[1][1], o[1][2], o[1][3]);
    const rsrc_t rw2 = mkrsrc(ws);
#pragma unroll
    for (int n3 = 0; n3 < 4; ++n3) {
      f32x4 w; w.xy = o[0][n3]; w.zw = o[1][n3];
      st16(rw2, (u32)WS_ZT + (u32)(kd * SEQ * 8 + (tid + 256 * n3) * 16), __builtin_bit_cast(u32x4, w));
    }
  }
  WAIT_V(0);
  SYNC();
  u32* sync = (u32*)(ws + WS_SYNC);
  if (tid == 0) {
    if (__hip_atomic_fetch_add(sync + (kd & 7) * 16, 1u, __ATOMIC_RELAXED, __HIP_MEMORY_SCOPE_AGENT) == 31)
      __hip_atomic_fetch_add(sync + 40 * 16, 1u, __ATOMIC_RELAXED, __HIP_MEMORY_SCOPE_AGENT);
  }
  TS(4);

  const int s0 = kd * 8;
  const int row = s0 + 2 * wave;
  const float g0 = ((const float*)(ws + WS_G))[row], g1 = ((const float*)(ws + WS_G))[row + 1];
  if (wave == 0) {
    for (int it = 0; it < (1 << 22); ++it) {
      if (__hip_atomic_load(sync + 40 * 16, __ATOMIC_RELAXED, __HIP_MEMORY_SCOPE_AGENT) >= 8) break;
      __builtin_amdgcn_s_sleep(4);
    }
  }
  SYNC();
  TS(5);
  const rsrc_t rws = mkrsrc(ws);
  u32x4 z[4];
#pragma unroll
  for (int r = 0; r < 4; ++r) {
    const int i = tid + 256 * r;
    z[r] = __builtin_amdgcn_raw_buffer_load_b128(rws, (u32)WS_ZT + (u32)((i >> 2) * SEQ + s0 + 2 * (i & 3)) * 8, 0, 16);
  }
#pragma unroll
  for (int r = 0; r < 4; ++r) {
    const int i = tid + 256 * r;
    const int kk = i >> 2, p = i & 3;
    const f32x4 qq = __builtin_bit_cast(f32x4, z[r]);
    f32x2 c1, c2;
    if (kk) {
      c1.x = qq.x - qq.w; c1.y = qq.y + qq.z;
      c2.x = qq.x + qq.w; c2.y = qq.z - qq.y;
    } else {
      c1.x = qq.x; c1.y = qq.z;
      c2.x = qq.y; c2.y = qq.w;
    }
    lds[p * PS3 + sslot(kk)] = c1;
    lds[p * PS3 + sslot(kk ? 512 - kk : 256)] = c2;
  }
  SYNC();
  TS(6);
  f32x2* buf3 = lds + wave * PS3;
#pragma unroll
  for (int k2 = 0; k2 < 8; ++k2) v[k2] = buf3[sslot(hi + 8 * lo + 64 * k2)];
  fft512_inv(v, buf3, tw, lane);
  wave_sync();
#pragma unroll
  for (int j = 0; j < 8; ++j) buf3[lane + 2 * (lane >> 3) + 80 * j] = v[j];
  wave_sync();
  f32x4 u[4];
#pragma unroll
  for (int j = 0; j < 4; ++j) u[j] = *(const f32x4*)(buf3 + 10 * lane + 2 * j);
  TS(7);
  for (int it = 0; it < (1 << 22); ++it) {
    if (__hip_atomic_load(sync + (8 + (s0 >> 6)) * 16, __ATOMIC_RELAXED, __HIP_MEMORY_SCOPE_AGENT) >= 8) break;
    __builtin_amdgcn_s_sleep(2);
  }
  asm volatile("" ::: "memory");
  TS(8);
  const u32x4 b0 = __builtin_amdgcn_raw_buffer_load_b128(rws, (u32)WS_B + (u32)(row * DM + lane * 8) * 2, 0, 16);
  const u32x4 b1 = __builtin_amdgcn_raw_buffer_load_b128(rws, (u32)WS_B + (u32)((row + 1) * DM + lane * 8) * 2, 0, 16);
  const float sc = 1.0f / (2048.0f * 512.0f);
  const float og0 = bfr(1.0f - g0), og1 = bfr(1.0f - g1);
  u32x4 o0, o1;
#pragma unroll
  for (int j = 0; j < 4; ++j) {
    const f32x2 bv0 = unpk(b0[j]), bv1 = unpk(b1[j]);
    o0[j] = pack2(bfr(g0 * bfr(u[j].x * sc)) + bfr(og0 * bv0.x), bfr(g0 * bfr(u[j].z * sc)) + bfr(og0 * bv0.y));
    o1[j] = pack2(bfr(g1 * bfr(u[j].y * sc)) + bfr(og1 * bv1.x), bfr(g1 * bfr(u[j].w * sc)) + bfr(og1 * bv1.y));
  }
  const rsrc_t ro = mkrsrc(out);
  st16(ro, (u32)(row * DM + lane * 8) * 2, o0);
  st16(ro, (u32)((row + 1) * DM + lane * 8) * 2, o1);
  TS(9);
}

NOPK __global__ __launch_bounds__(256, 2) void k_l2(char* __restrict__ ws, u16* __restrict__ out) {
  extern __shared__ __attribute__((aligned(16))) char smem[];
  if (BIDX >= 256) g2_body(ws, smem, BIDX - 256);
  else k2_body(ws, smem, BIDX, out);
}

extern "C" void kernel_launch(void* const* d_in, const int* in_sizes, int n_in, void* d_out, int out_size,
                              void* d_ws, size_t ws_size, hipStream_t stream) {
  const u16* x = (const u16*)d_in[0];
  const u16* wr = (const u16*)d_in[1];
  const u16* wi = (const u16*)d_in[2];
  const u16* wg = (const u16*)d_in[4];
  const u16* wu = (const u16*)d_in[5];
  const u16* wd = (const u16*)d_in[6];
  const u16* gw = (const u16*)d_in[7];
  char* ws = (char*)d_ws;
  (void)hipFuncSetAttribute((const void*)k_l1, hipFuncAttributeMaxDynamicSharedMemorySize, L1_LDS);
  (void)hipFuncSetAttribute((const void*)k_l2, hipFuncAttributeMaxDynamicSharedMemorySize, L2_LDS);
  k_l1<<<256, 1024, L1_LDS, stream>>>(x, wr, wi, wg, wu, wd, gw, ws);
  k_l2<<<512, 256, L2_LDS, stream>>>(ws, (u16*)d_out);
}
```

```cpp
#include <hip/hip_runtime.h>
#include <hip/hip_bf16.h>
#include <stdint.h>

#if defined(__HIP_DEVICE_COMPILE__)
#define NOPK __attribute__((target("no-packed-fp32-ops")))
#else
#define NOPK
#endif
#define DEVI NOPK __device__ __forceinline__
#define TS(i)

typedef __attribute__((ext_vector_type(8))) short bf16x8;
typedef __attribute__((ext_vector_type(4))) short bf16x4;
typedef __attribute__((ext_vector_type(4))) float f32x4;
typedef __attribute__((ext_vector_type(2))) float f32x2;
typedef unsigned short u16;
typedef unsigned int u32;
typedef __attribute__((ext_vector_type(4))) unsigned int u32x4;
typedef __attribute__((ext_vector_type(2))) unsigned int u32x2;
typedef __attribute__((address_space(3))) bf16x4* lds_b4_ptr;
#define TIDX ((int)__builtin_amdgcn_workitem_id_x())
#define BIDX ((int)__builtin_amdgcn_workgroup_id_x())
#define U2F(x) __builtin_bit_cast(float, (u32)(x))
#define SYNC() do { __builtin_amdgcn_fence(__ATOMIC_RELEASE, "workgroup"); __builtin_amdgcn_s_barrier(); \
                    __builtin_amdgcn_fence(__ATOMIC_ACQUIRE, "workgroup"); } while (0)
DEVI float fexp(float x) { return __builtin_amdgcn_exp2f(x * 1.44269504088896340736f); }
DEVI float shfl_xor(float v, int m, int lane) {
  return __builtin_bit_cast(float, __builtin_amdgcn_ds_bpermute((lane ^ m) << 2, __builtin_bit_cast(int, v)));
}

constexpr int SEQ = 2048, DM = 512, DF = 257, FF = 1376;
constexpr int KS32 = FF / 32;

constexpr size_t WS_YT = 0;
constexpr size_t WS_WT = WS_YT + (size_t)DF * SEQ * 8;
constexpr size_t WS_H = WS_WT + (size_t)DF * SEQ * 4;
constexpr size_t WS_WD = WS_H + (size_t)SEQ * FF * 2;
constexpr size_t WS_B = WS_WD + (size_t)FF * DM * 2;
constexpr size_t WS_G = WS_B + (size_t)SEQ * DM * 2;
constexpr size_t WS_SYNC = WS_G + 8192;
constexpr size_t WS_ZT = WS_SYNC + 4096;

DEVI float bf2f(u16 v) { return U2F(((u32)v) << 16); }
DEVI u16 f2bf(float f) {
  __bf16 b = (__bf16)f;
  return __builtin_bit_cast(u16, b);
}
DEVI float bfr(float f) { return bf2f(f2bf(f)); }

DEVI f32x2 cmul(f32x2 a, f32x2 b) {
  f32x2 r;
  r.x = a.x * b.x - a.y * b.y;
  r.y = a.x * b.y + a.y * b.x;
  return r;
}
DEVI f32x2 cmulc(f32x2 a, f32x2 b) {
  f32x2 r;
  r.x = a.x * b.x + a.y * b.y;
  r.y = a.y * b.x - a.x * b.y;
  return r;
}
DEVI f32x2 twid(float frac) {
  f32x2 r;
  r.x = __builtin_amdgcn_cosf(frac);
  r.y = -__builtin_amdgcn_sinf(frac);
  return r;
}

template <int SGN>
DEVI f32x2 mul_i(f32x2 z) {
  f32x2 r;
  if (SGN < 0) { r.x = z.y; r.y = -z.x; } else { r.x = -z.y; r.y = z.x; }
  return r;
}
template <int SGN>
DEVI void dft4(f32x2 b0, f32x2 b1, f32x2 b2, f32x2 b3, f32x2& y0, f32x2& y1, f32x2& y2,
                                     f32x2& y3) {
  f32x2 c0 = b0 + b2, c1 = b0 - b2, c2 = b1 + b3, c3 = mul_i<SGN>(b1 - b3);
  y0 = c0 + c2; y2 = c0 - c2; y1 = c1 + c3; y3 = c1 - c3;
}
template <int SGN>
DEVI void dft8(f32x2 (&v)[8]) {
  const float R = 0.70710678118654752440f;
  f32x2 a0 = v[0] + v[4], a4 = v[0] - v[4];
  f32x2 a1 = v[1] + v[5], a5 = v[1] - v[5];
  f32x2 a2 = v[2] + v[6], a6 = v[2] - v[6];
  f32x2 a3 = v[3] + v[7], a7 = v[3] - v[7];
  f32x2 t5, t7;
  if (SGN < 0) {
    t5.x = (a5.x + a5.y) * R; t5.y = (a5.y - a5.x) * R;
    t7.x = (a7.y - a7.x) * R; t7.y = -(a7.x + a7.y) * R;
  } else {
    t5.x = (a5.x - a5.y) * R; t5.y = (a5.x + a5.y) * R;
    t7.x = -(a7.x + a7.y) * R; t7.y = (a7.x - a7.y) * R;
  }
  f32x2 t6 = mul_i<SGN>(a6);
  dft4<SGN>(a0, a1, a2, a3, v[0], v[2], v[4], v[6]);
  dft4<SGN>(a4, t5, t6, t7, v[1], v[3], v[5], v[7]);
}

DEVI void wave_sync() {
  asm volatile("s_waitcnt lgkmcnt(0)" ::: "memory");
  __builtin_amdgcn_wave_barrier();
}

struct Tw512 {
  f32x2 a[7];
  f32x2 b[7];
};
DEVI void make_tw512(Tw512& t, int lane) {
#pragma unroll
  for (int k = 1; k < 8; ++k) {
    t.a[k - 1] = twid((float)((lane * k) & 511) * (1.0f / 512.0f));
    t.b[k - 1] = twid((float)(((lane & 7) * k) & 63) * (1.0f / 64.0f));
  }

}

DEVI void fft512_fwd(f32x2 (&v)[8], f32x2* buf, const Tw512& tw, int lane) {
  const int hi = lane >> 3, lo = lane & 7;
  dft8<-1>(v);
#pragma unroll
  for (int k = 1; k < 8; ++k) v[k] = cmul(v[k], tw.a[k - 1]);
  wave_sync();
#pragma unroll
  for (int k = 0; k < 8; ++k) buf[72 * k + lane] = v[k];
  wave_sync();
#pragma unroll
  for (int n = 0; n < 8; ++n) v[n] = buf[72 * hi + 8 * n + lo];
  dft8<-1>(v);
#pragma unroll
  for (int k = 1; k < 8; ++k) v[k] = cmul(v[k], tw.b[k - 1]);
  wave_sync();
#pragma unroll
  for (int k = 0; k < 8; ++k) buf[72 * hi + 9 * k + lo] = v[k];
  wave_sync();
#pragma unroll
  for (int n = 0; n < 8; ++n) v[n] = buf[72 * hi + 9 * lo + n];
  dft8<-1>(v);
}
DEVI void fft512_inv(f32x2 (&v)[8], f32x2* buf, const Tw512& tw, int lane) {
  const int hi = lane >> 3, lo = lane & 7;
  dft8<1>(v);
  wave_sync();
#pragma unroll
  for (int n = 0; n < 8; ++n) buf[72 * hi + 9 * lo + n] = v[n];
  wave_sync();
#pragma unroll
  for (int k = 0; k < 8; ++k) v[k] = buf[72 * hi + 9 * k + lo];
#pragma unroll
  for (int k = 1; k < 8; ++k) v[k] = cmulc(v[k], tw.b[k - 1]);
  dft8<1>(v);
  wave_sync();
#pragma unroll
  for (int n = 0; n < 8; ++n) buf[72 * hi + 8 * n + lo] = v[n];
  wave_sync();
#pragma unroll
  for (int k = 0; k < 8; ++k) v[k] = buf[72 * k + lane];
#pragma unroll
  for (int k = 1; k < 8; ++k) v[k] = cmulc(v[k], tw.a[k - 1]);
  dft8<1>(v);
}

DEVI int sslot(int k) { return k + 4 * (k >> 5); }
constexpr int PS = 584;

#define PREFETCH 0
#define AUX_GATE 7
constexpr int G1_NS = 3;
constexpr int G1_X_OFF = G1_NS * 24576;
constexpr int AX_OFF = G1_X_OFF + G1_NS * 16384;
constexpr int AX_W_OFF = AX_OFF + 4 * PS * 8;
constexpr int AX_WD_OFF = AX_W_OFF + 10240;
constexpr int AX_GW_OFF = AX_WD_OFF;
constexpr int L1_CNT_OFF = AX_WD_OFF + 8192;
constexpr int L1_LDS = L1_CNT_OFF + 256;

#define WAIT_V(n) asm volatile("s_waitcnt vmcnt(" #n ")" ::: "memory")
#define WAIT_L(n) asm volatile("s_waitcnt lgkmcnt(" #n ")" ::: "memory")
#define RAW_BAR() asm volatile("s_barrier" ::: "memory")

typedef __amdgpu_buffer_rsrc_t rsrc_t;
typedef __attribute__((ext_vector_type(4))) int i32x4;
DEVI void dma16(const void* gsrc, u32 lds_dst) {
  u32 keep;
  asm volatile("s_mov_b32 %0, m0\n\ts_mov_b32 m0, %2\n\ts_nop 0\n\tglobal_load_lds_dwordx4 %1, off\n\ts_mov_b32 m0, %0"
               : "=&s"(keep) : "v"(gsrc), "s"(lds_dst) : "memory");
}
DEVI void dma16b(i32x4 rs, u32 voff, u32 soff, u32 lds_dst) {
  u32 keep;
  asm volatile("s_mov_b32 %0, m0\n\ts_mov_b32 m0, %4\n\ts_nop 0\n\tbuffer_load_dwordx4 %1, %2, %3 offen lds\n\ts_mov_b32 m0, %0"
               : "=&s"(keep) : "v"(voff), "s"(rs), "s"(soff), "s"(lds_dst) : "memory");
}
DEVI i32x4 mkrs(const void* p, u32 bytes) {
  const unsigned long long a = (unsigned long long)p;
  i32x4 r;
  r.x = __builtin_amdgcn_readfirstlane((int)(u32)a);
  r.y = __builtin_amdgcn_readfirstlane((int)(u32)(a >> 32) & 0xffff);
  r.z = (int)bytes;
  r.w = 0x00020000;
  return r;
}
DEVI rsrc_t mkrsrc(const void* p) { return __builtin_amdgcn_make_buffer_rsrc((void*)p, 0, 0x7fffffff, 0x00020000); }
DEVI void st16(rsrc_t r, u32 off, u32x4 v) { __builtin_amdgcn_raw_buffer_store_b128(v, r, off, 0, 16); }
DEVI u32 pack2(float a, float b) { return (u32)f2bf(a) | ((u32)f2bf(b) << 16); }

typedef __attribute__((address_space(3))) u32* lds_u32_ptr;
DEVI void lds_signal(u32 addr, int lane) {
  unsigned long long keep;
  asm volatile("s_mov_b64 %0, exec\n\ts_mov_b64 exec, 1\n\tds_add_u32 %1, %2\n\ts_mov_b64 exec, %0"
               : "=&s"(keep) : "v"(addr), "v"(1u) : "memory");
}
DEVI void lds_wait1(u32 addr, u32 need) {
  while (true) {
    u32 v;
    asm volatile("ds_read_b32 %0, %1\n\ts_waitcnt lgkmcnt(0)" : "=v"(v) : "v"(addr) : "memory");
    if ((u32)__builtin_amdgcn_readfirstlane(v) >= need) break;
    __builtin_amdgcn_s_sleep(1);
  }
}
DEVI void lds_wait2(u32 addr, u32 need0, u32 need1) {
  while (true) {
    u32x2 v;
    asm volatile("ds_read_b64 %0, %1\n\ts_waitcnt lgkmcnt(0)" : "=v"(v) : "v"(addr) : "memory");
    const u32 a = __builtin_amdgcn_readfirstlane(v.x), b = __builtin_amdgcn_readfirstlane(v.y);
    if (a >= need0 && b >= need1) break;
    __builtin_amdgcn_s_sleep(1);
  }
}

DEVI void aux_body(const u16* __restrict__ x, const u16* __restrict__ wr, const u16* __restrict__ wi,
                   const u16* __restrict__ wd, const u16* __restrict__ gw, char* __restrict__ ws, char* smem, int a,
                   int wave, int lane, bool gated) {
  constexpr int TSK = 0;
  const u32 lds0 = (u32)(size_t)(__attribute__((address_space(3))) char*)smem;
  const rsrc_t rws = mkrsrc(ws);
  const int s0 = a * 8;
  const int tid = wave * 64 + lane;
  f32x2* aux = (f32x2*)(smem + AX_OFF);
  {
    const int p = wave;
    Tw512 tw;
    make_tw512(tw, lane);
    WAIT_V(0);
    TS(1);
    const u16* xa = (const u16*)(aux + p * PS);
    const u16* gl = (const u16*)(smem + AX_GW_OFF + p * 1024);
    u16 ra[8], rb[8], rg[8];
#pragma unroll
    for (int j = 0; j < 8; ++j) {
      ra[j] = xa[lane + 64 * j];
      rb[j] = xa[DM + lane + 64 * j];
      rg[j] = gl[lane + 64 * j];
    }
    f32x2 v[8];
    float sa = 0.f, sb = 0.f;
#pragma unroll
    for (int j = 0; j < 8; ++j) {
      v[j].x = bf2f(ra[j]);
      v[j].y = bf2f(rb[j]);
      float w = bf2f(rg[j]);
      sa += bfr(v[j].x * w);
      sb += bfr(v[j].y * w);
    }
#pragma unroll
    for (int o = 32; o > 0; o >>= 1) {
      sa += shfl_xor(sa, o, lane);
      sb += shfl_xor(sb, o, lane);
    }
    if (lane < 2) {
      float s = bfr(lane ? sb : sa);
      float gg = bfr(1.0f / (1.0f + fexp(-s)));
      ((float*)(ws + WS_G))[s0 + 2 * p + lane] = gg;
    }
    f32x2* buf = aux + p * PS;
    fft512_fwd(v, buf, tw, lane);
    wave_sync();
    const int hi = lane >> 3, lo = lane & 7;
#pragma unroll
    for (int k2 = 0; k2 < 8; ++k2) buf[sslot(hi + 8 * lo + 64 * k2)] = v[k2];
  }
  WAIT_L(0);
  lds_signal(lds0 + L1_CNT_OFF + 42 * 4, lane);
  TS(2);
  if (gated) lds_wait1(lds0 + L1_CNT_OFF + 44 * 4, 4);
  {
    const int p = wave;
    const char* pr = (const char*)wr + (size_t)a * 4112;
    const char* pi = (const char*)wi + (size_t)a * 4112;
    dma16(pr + p * 1024 + lane * 16, lds0 + AX_W_OFF + p * 1024);
    dma16(pi + p * 1024 + lane * 16, lds0 + AX_W_OFF + 5120 + p * 1024);
    if (p < 2) dma16((p ? pi : pr) + (lane == 0 ? 4096 : 0), lds0 + AX_W_OFF + p * 5120 + 4096);
    const char* pd = (const char*)wd + (size_t)(a < 172 ? a : 0) * 8192;
    dma16(pd + p * 1024 + lane * 16, lds0 + AX_WD_OFF + p * 1024);
    dma16(pd + 4096 + p * 1024 + lane * 16, lds0 + AX_WD_OFF + 4096 + p * 1024);
  }
  lds_wait1(lds0 + L1_CNT_OFF + 42 * 4, 4);
#pragma unroll
  for (int r = 0; r < 4; ++r) {
    const int i = tid + 256 * r;
    const int kd = i >> 2, p = i & 3;
    const f32x2 z1 = aux[p * PS + sslot(kd)];
    const f32x2 z2 = aux[p * PS + sslot(kd ? 512 - kd : 256)];
    f32x4 o;
    if (kd) {
      o.x = 0.5f * (z1.x + z2.x);
      o.y = 0.5f * (z1.y - z2.y);
      o.z = 0.5f * (z1.y + z2.y);
      o.w = 0.5f * (z2.x - z1.x);
    } else {
      o.x = z1.x; o.y = z2.x; o.z = z1.y; o.w = z2.y;
    }
    st16(rws, (u32)WS_YT + (u32)(kd * SEQ + s0 + 2 * p) * 8, __builtin_bit_cast(u32x4, o));
  }
  TS(3);
  WAIT_V(4);
  lds_signal(lds0 + L1_CNT_OFF + 43 * 4, lane);
  lds_wait1(lds0 + L1_CNT_OFF + 43 * 4, 4);
  {
    const u16* lr = (const u16*)(smem + AX_W_OFF);
    const u16* li = (const u16*)(smem + AX_W_OFF + 5120);
#pragma unroll
    for (int r = 0; r < 3; ++r) {
      const int i = tid + 256 * r;
      if (i < DF * 2) {
        const int kd = i >> 1, h = i & 1;
        u32x4 o;
#pragma unroll
        for (int q = 0; q < 4; ++q) o[q] = (u32)lr[(h * 4 + q) * DF + kd] | ((u32)li[(h * 4 + q) * DF + kd] << 16);
        st16(rws, (u32)WS_WT + (u32)(kd * SEQ + s0 + h * 4) * 4, o);
      }
    }
  }
  if (a < 172) {
    const u16* ld = (const u16*)(smem + AX_WD_OFF);
#pragma unroll
    for (int r = 0; r < 2; ++r) {
      const int n = tid + 256 * r;
      u32x4 o;
#pragma unroll
      for (int q = 0; q < 4; ++q) o[q] = (u32)ld[(2 * q) * DM + n] | ((u32)ld[(2 * q + 1) * DM + n] << 16);
      const int ks = a >> 2, kq = a & 3;
      st16(rws, (u32)WS_WD + (u32)(((ks * 32 + (n >> 4)) * 64 + (n & 15) + 16 * kq) * 16), o);
    }
  }
  TS(4);
}

NOPK __global__ __launch_bounds__(1024, 4) void k_l1(const u16* __restrict__ x, const u16* __restrict__ wr,
                                                const u16* __restrict__ wi, const u16* __restrict__ wg,
                                                const u16* __restrict__ wu, const u16* __restrict__ wd,
                                                const u16* __restrict__ gw, char* __restrict__ ws) {
  extern __shared__ __attribute__((aligned(16))) char smem[];
  const int bid = BIDX;
  const int tid = TIDX, lane = tid & 63;
  const int wave = __builtin_amdgcn_readfirstlane(tid >> 6);
  constexpr int TSK = 0;
  TS(0);
  const u32 lds0 = (u32)(size_t)(__attribute__((address_space(3))) char*)smem;
  const u32 cnt = lds0 + L1_CNT_OFF;
  if (wave == 0) { ((u32*)(smem + L1_CNT_OFF))[lane] = 0; WAIT_L(0); }
  RAW_BAR();
  const int xcd = bid & 7, idx = bid >> 3;
  const int T = xcd * 30 + (idx < 30 ? idx : 0);
  const int fg = T / 80, rem = T - fg * 80;
  const int mt_ = rem / 5, fi = rem - mt_ * 5;
  const int m0 = mt_ * 128, f0 = (fg * 5 + fi) * 96;
  if (wave >= 12) {
    __builtin_amdgcn_s_setprio(2);
    const int w = wave - 12;
    if (bid == 255 && w == 0) ((u32*)(ws + WS_SYNC))[lane * 16] = 0;
    const char* px = (const char*)x + (size_t)(bid * 8 + 2 * w) * 1024;
    dma16(px + lane * 16, lds0 + AX_OFF + w * (PS * 8));
    dma16(px + 1024 + lane * 16, lds0 + AX_OFF + w * (PS * 8) + 1024);
    dma16((const char*)gw + lane * 16, lds0 + AX_GW_OFF + w * 1024);
    aux_body(x, wr, wi, wd, gw, ws, smem, bid, w, lane, idx < 30);
    return;
  }
  if (idx >= 30) return;

  {
    const int q4 = wave >= 8 ? wave - 8 : (wave >> 1);
    const i32x4 rx = mkrs(x, SEQ * DM * 2), rg = mkrs(wg, DM * FF * 2), ru = mkrs(wu, DM * FF * 2);
    u32 vx, vw[3];
    {
      const int row = lane >> 3;
      const int c = (lane & 7) ^ row;
      vx = (u32)((m0 + q4 * 32 + row) * DM + c * 8) * 2;
#pragma unroll
      for (int j = 0; j < 3; ++j) {
        const int q = 64 * j + lane;
        const int k = q / 12, cp = q - k * 12;
        const int c2 = cp ^ (((k >> 3) & 1) << 1);
        vw[j] = (u32)((q4 * 16 + k) * FF + f0 + c2 * 8) * 2;
      }
    }
    auto issue = [&](int s, int lo, int hi) NOPK __attribute__((always_inline)) {
      const int slot = s % G1_NS;
      const u32 wdst = lds0 + slot * 24576 + q4 * 3072, xdst = lds0 + G1_X_OFF + slot * 16384 + q4 * 4096;
      const u32 sw = s * (64 * FF * 2);
#pragma unroll
      for (int n = 0; n < 10; ++n) {
        if (n >= lo && n < hi) {
          if (n < 3) dma16b(rg, vw[n], sw, wdst + n * 1024);
          else if (n < 7) dma16b(rx, vx, s * 128 + (n - 3) * 8192, xdst + (n - 3) * 1024);
          else dma16b(ru, vw[n - 7], sw, wdst + 12288 + (n - 7) * 1024);
        }
      }
    };
    if (wave >= 8) {
      issue(1, 0, 10);
      issue(2, 0, 5);
      WAIT_V(5);
      lds_signal(cnt + 1 * 4, lane);
      issue(2, 5, 10);
#pragma unroll 1
      for (int s = 3; s < 8; ++s) {
        lds_wait1(cnt + (16 + s - 3) * 4, 8);
        issue(s, 0, 3);
        WAIT_V(3);
        lds_signal(cnt + (s - 1) * 4, lane);
        issue(s, 3, 10);
        if (s == AUX_GATE) lds_signal(cnt + 44 * 4, lane);
      }
      WAIT_V(0);
      lds_signal(cnt + 7 * 4, lane);
      return;
    }
    if (wave & 1) issue(0, 5, 10); else issue(0, 0, 5);
    WAIT_V(0);
    lds_signal(cnt, lane);
  }

  const int wm = wave >> 1, wf = wave & 1;
  const int l15 = lane & 15, lq = lane >> 4;
  int xoff[2][2];
#pragma unroll
  for (int mt = 0; mt < 2; ++mt)
#pragma unroll
    for (int kk = 0; kk < 2; ++kk) {
      const int row = wm * 32 + mt * 16 + l15;
      xoff[mt][kk] = G1_X_OFF + row * 128 + (((kk * 4 + lq) ^ (row & 7)) << 4);
    }
  const int wq = l15 >> 2, wp = l15 & 3;
  int woff[3];
#pragma unroll
  for (int ft = 0; ft < 3; ++ft)
    woff[ft] = (8 * lq + wq) * 192 + (((2 * (wf * 3 + ft) + (wp >> 1)) ^ (2 * (lq & 1))) << 4) + (wp & 1) * 8;

  f32x4 accg[3][2], accu[3][2];
#pragma unroll
  for (int a = 0; a < 3; ++a)
#pragma unroll
    for (int b = 0; b < 2; ++b) { accg[a][b] = (f32x4)0.f; accu[a][b] = (f32x4)0.f; }

  bf16x8 xa[2], xb[2], wgf[3], wuf[3];
  auto rdx = [&](bf16x8(&xf)[2], int xs, int kk) NOPK __attribute__((always_inline)) {
#pragma unroll
    for (int mt = 0; mt < 2; ++mt) xf[mt] = *(const bf16x8*)(smem + xs + xoff[mt][kk]);
  };
  auto rdw = [&](bf16x8(&wfr)[3], int wsl, int kk, int mat) NOPK __attribute__((always_inline)) {
#pragma unroll
    for (int n = 0; n < 3; ++n) {
      const char* base = smem + (wsl + woff[n]) + kk * 6144 + mat * 12288;
      bf16x4 lo = __builtin_amdgcn_ds_read_tr16_b64_v4i16((lds_b4_ptr)(base));
      bf16x4 hi = __builtin_amdgcn_ds_read_tr16_b64_v4i16((lds_b4_ptr)(base + 768));
      wfr[n] = __builtin_shufflevector(lo, hi, 0, 1, 2, 3, 4, 5, 6, 7);
    }
  };
  auto mma = [&](f32x4(&acc)[3][2], bf16x8(&xf)[2], bf16x8(&wfr)[3]) NOPK __attribute__((always_inline)) {
#pragma unroll
    for (int ft = 0; ft < 3; ++ft)
#pragma unroll
      for (int mt = 0; mt < 2; ++mt)
        acc[ft][mt] = __builtin_amdgcn_mfma_f32_16x16x32_bf16(wfr[ft], xf[mt], acc[ft][mt], 0, 0, 0);
  };
  auto interleave = [&]() NOPK __attribute__((always_inline)) {
#pragma unroll
    for (int i = 0; i < 6; ++i) {
      __builtin_amdgcn_sched_group_barrier(0x008, 1, 0);
      __builtin_amdgcn_sched_group_barrier(0x100, 2, 0);
    }
  };
  const volatile u32* cv = (const volatile u32*)(smem + L1_CNT_OFF);

  TS(1);
  lds_wait1(cnt, 8);
  TS(4);
  rdx(xa, 0, 0);
  rdw(wgf, 0, 0, 0);
#define SB() __builtin_amdgcn_sched_barrier(0)
  int slot = 0;
#pragma unroll 1
  for (int S = 0; S < 8; ++S) {
    const int wsl = slot * 24576, xs = slot * 16384;
    slot = slot == G1_NS - 1 ? 0 : slot + 1;
    const int wsn = slot * 24576, xn = slot * 16384;
    SB(); rdw(wuf, wsl, 0, 1); mma(accg, xa, wgf); interleave();
    SB(); rdw(wgf, wsl, 1, 0); rdx(xb, xs, 1); mma(accu, xa, wuf); interleave();
    SB(); rdw(wuf, wsl, 1, 1); const u32 fv = cv[S + 1]; mma(accg, xb, wgf); interleave();
    SB();
    WAIT_L(0);
    lds_signal(cnt + (16 + S) * 4, lane);
    if (S < 7) {
      if ((u32)__builtin_amdgcn_readfirstlane(fv) < 4) lds_wait1(cnt + (S + 1) * 4, 4);
    }
    SB();
    rdw(wgf, wsn, 0, 0); rdx(xa, xn, 0);
    mma(accu, xb, wuf);
    interleave();
  }
  SB();
  TS(5);
  const rsrc_t rws = mkrsrc(ws);
  const int lane2 = __builtin_amdgcn_mbcnt_hi(~0u, __builtin_amdgcn_mbcnt_lo(~0u, 0u));
  const int e15 = lane2 & 15, eq = lane2 >> 4;
#pragma unroll
  for (int ft = 0; ft < 3; ++ft) {
    u32 pk[2][2];
#pragma unroll
    for (int mt = 0; mt < 2; ++mt) {
      float o[4];
#pragma unroll
      for (int r = 0; r < 4; ++r) {
        const float g = bfr(accg[ft][mt][r]);
        const float sl = bfr(g * __builtin_amdgcn_rcpf(1.0f + fexp(-g)));
        const float u = bfr(accu[ft][mt][r]);
        o[r] = sl * u;
      }
      pk[mt][0] = pack2(o[0], o[1]);
      pk[mt][1] = pack2(o[2], o[3]);
    }
    auto s0_ = __builtin_amdgcn_permlane16_swap(pk[0][0], pk[1][0], false, false);
    auto s1_ = __builtin_amdgcn_permlane16_swap(pk[0][1], pk[1][1], false, false);
    u32x4 ov;
    ov.x = s0_[0]; ov.y = s1_[0]; ov.z = s0_[1]; ov.w = s1_[1];
    const int f = f0 + (wf * 3 + ft) * 16 + 8 * (eq >> 1);
    const int m = m0 + wm * 32 + (eq & 1) * 16 + e15;
    if (f < FF)
      st16(rws, (u32)WS_H + (u32)(((m >> 4) * KS32 + (f >> 5)) * 1024 + ((m & 15) + 16 * ((f & 31) >> 3)) * 16), ov);
  }
  TS(6);
}

constexpr int L2_LDS = 4 * PS * 8 + 32768;
constexpr int G2_DEPTH = 4;

DEVI void g2_body(char* __restrict__ ws, char* smem, int bid) {
  const int tid = TIDX, lane = tid & 63;
  const int wave = __builtin_amdgcn_readfirstlane(tid >> 6);
  constexpr int TSK = 1;
  TS(0);
  const int xcd = bid & 7, idx = bid >> 3;
  const int mt64 = xcd * 4 + (idx >> 3), nt64 = idx & 7;
  const char* hb = ws + WS_H + (size_t)(mt64 * 4) * KS32 * 1024 + lane * 16;
  const char* wb = ws + WS_WD + (size_t)(nt64 * 4) * 1024 + lane * 16;
  f32x4 acc[4][4];
#pragma unroll
  for (int a = 0; a < 4; ++a)
#pragma unroll
    for (int b = 0; b < 4; ++b) acc[a][b] = (f32x4)0.f;
  constexpr int NS = G2_DEPTH + 1;
  bf16x8 hf[NS][4], wf[NS][4];
  auto ld = [&](int slot, int i) NOPK __attribute__((always_inline)) {
    int ks = wave + 4 * i;
    ks = ks > KS32 - 1 ? KS32 - 1 : ks;
#pragma unroll
    for (int j = 0; j < 4; ++j) {
      hf[slot][j] = *(const bf16x8*)(hb + ((size_t)j * KS32 + ks) * 1024);
      wf[slot][j] = *(const bf16x8*)(wb + ((size_t)ks * 32 + j) * 1024);
    }
  };
#pragma unroll
  for (int i = 0; i < G2_DEPTH; ++i) ld(i, i);
  __builtin_amdgcn_sched_barrier(0);
#pragma unroll
  for (int i = 0; i < 11; ++i) {
    if (i + G2_DEPTH < 11) ld((i + G2_DEPTH) % NS, i + G2_DEPTH);
    __builtin_amdgcn_sched_barrier(0);
    if (i < 10 || wave < 3) {
#pragma unroll
      for (int nt = 0; nt < 4; ++nt)
#pragma unroll
        for (int mt = 0; mt < 4; ++mt)
          acc[nt][mt] = __builtin_amdgcn_mfma_f32_16x16x32_bf16(wf[i % NS][nt], hf[i % NS][mt], acc[nt][mt], 0, 0, 0);
    }
    __builtin_amdgcn_sched_barrier(0);
  }
  TS(1);
  f32x4* red = (f32x4*)smem;
#pragma unroll
  for (int nt = 0; nt < 4; ++nt) {
    if (nt != wave) {
      const int src = wave < nt ? wave : wave - 1;
#pragma unroll
      for (int mt = 0; mt < 4; ++mt) red[((nt * 3 + src) * 4 + mt) * 64 + lane] = acc[nt][mt];
    }
  }
  SYNC();
  f32x4 fin[4];
#pragma unroll
  for (int nt = 0; nt < 4; ++nt)
    if (nt == wave) {
#pragma unroll
      for (int mt = 0; mt < 4; ++mt) fin[mt] = acc[nt][mt];
    }
  const rsrc_t rws = mkrsrc(ws);
  const int l15 = lane & 15, lq = lane >> 4;
#pragma unroll
  for (int mt = 0; mt < 4; ++mt)
#pragma unroll
    for (int s = 0; s < 3; ++s) fin[mt] += red[((wave * 3 + s) * 4 + mt) * 64 + lane];
#pragma unroll
  for (int pr = 0; pr < 2; ++pr) {
    auto s0_ = __builtin_amdgcn_permlane16_swap(pack2(fin[2 * pr][0], fin[2 * pr][1]),
                                                pack2(fin[2 * pr + 1][0], fin[2 * pr + 1][1]), false, false);
    auto s1_ = __builtin_amdgcn_permlane16_swap(pack2(fin[2 * pr][2], fin[2 * pr][3]),
                                                pack2(fin[2 * pr + 1][2], fin[2 * pr + 1][3]), false, false);
    u32x4 ov;
    ov.x = s0_[0]; ov.y = s1_[0]; ov.z = s0_[1]; ov.w = s1_[1];
    const int m = mt64 * 64 + (2 * pr + (lq & 1)) * 16 + l15;
    const int n = nt64 * 64 + wave * 16 + 8 * (lq >> 1);
    st16(rws, (u32)WS_B + (u32)(m * DM + n) * 2, ov);
  }
  WAIT_V(0);
  SYNC();
  if (tid == 0) __hip_atomic_fetch_add((u32*)(ws + WS_SYNC) + (8 + mt64) * 16, 1u, __ATOMIC_RELAXED, __HIP_MEMORY_SCOPE_AGENT);
  TS(2);
}

DEVI f32x2 unpk(u32 w) {
  f32x2 r;
  r.x = U2F(w << 16);
  r.y = U2F(w & 0xffff0000u);
  return r;
}

constexpr int PS3 = 648;
DEVI void k2_body(char* __restrict__ ws, char* smem, int kd, u16* __restrict__ out) {
  const int tid = TIDX, lane = tid & 63;
  const int wave = __builtin_amdgcn_readfirstlane(tid >> 6);
  constexpr int TSK = 1;
  TS(0);
  const f32x4* col = (const f32x4*)(ws + WS_YT + (size_t)kd * SEQ * 8);
  const u32* wt = (const u32*)(ws + WS_WT) + (size_t)kd * SEQ;
  f32x2* lds = (f32x2*)smem;
  f32x4 q[4];
#pragma unroll
  for (int n3 = 0; n3 < 4; ++n3) q[n3] = col[tid + 256 * n3];
  const int hi = lane >> 3, lo = lane & 7;
  const int kbase = wave + 4 * (hi + 8 * lo);
  u32 wv[8];
#pragma unroll
  for (int k2 = 0; k2 < 8; ++k2) wv[k2] = wt[kbase + 256 * k2];
  u32 wx[8];
  if (kd == 0) {
    const u32* wn = (const u32*)(ws + WS_WT) + (size_t)256 * SEQ;
#pragma unroll
    for (int k2 = 0; k2 < 8; ++k2) wx[k2] = wn[kbase + 256 * k2];
  }
  Tw512 tw;
  make_tw512(tw, lane);
  f32x2 t4[2][3];
#pragma unroll
  for (int h = 0; h < 2; ++h)
#pragma unroll
    for (int k = 1; k < 4; ++k) t4[h][k - 1] = twid((float)(((2 * tid + h) * k) & 2047) * (1.0f / 2048.0f));
  {
    f32x2 y[2][4];
    dft4<-1>(q[0].xy, q[1].xy, q[2].xy, q[3].xy, y[0][0], y[0][1], y[0][2], y[0][3]);
    dft4<-1>(q[0].zw, q[1].zw, q[2].zw, q[3].zw, y[1][0], y[1][1], y[1][2], y[1][3]);
#pragma unroll
    for (int k = 0; k < 4; ++k) {
      f32x2 a = y[0][k], b = y[1][k];
      if (k) { a = cmul(a, t4[0][k - 1]); b = cmul(b, t4[1][k - 1]); }
      f32x4 o; o.xy = a; o.zw = b;
      *(f32x4*)(lds + k * PS + 2 * tid) = o;
    }
  }
  SYNC();
  TS(1);
  f32x2* buf = lds + wave * PS;
  f32x2 v[8];
#pragma unroll
  for (int j = 0; j < 8; ++j) v[j] = buf[lane + 64 * j];
  fft512_fwd(v, buf, tw, lane);
  TS(2);
  if (kd != 0) {
#pragma unroll
    for (int k2 = 0; k2 < 8; ++k2) v[k2] = cmul(v[k2], unpk(wv[k2]));
  } else {
    f32x4* ex = (f32x4*)(lds + 4 * PS);
#pragma unroll
    for (int k2 = 0; k2 < 8; ++k2) {
      f32x4 e; e.xy = v[k2]; e.z = U2F(wv[k2]); e.w = U2F(wx[k2]);
      ex[kbase + 256 * k2] = e;
    }
    SYNC();
#pragma unroll
    for (int k2 = 0; k2 < 8; ++k2) {
      const int k = kbase + 256 * k2, kn = (2048 - k) & 2047;
      const f32x4 e = ex[kn];
      const f32x2 c1 = v[k2], c2 = e.xy;
      f32x2 r0, r1;
      r0.x = 0.5f * (c1.x + c2.x); r0.y = 0.5f * (c1.y - c2.y);
      r1.x = 0.5f * (c1.y + c2.y); r1.y = 0.5f * (c2.x - c1.x);
      const u32x4 eu = __builtin_bit_cast(u32x4, e);
      const u32 ez = eu.z, ew = eu.w;
      const f32x2 a1 = unpk(wv[k2]), a2 = unpk(ez), b1 = unpk(wx[k2]), b2 = unpk(ew);
      f32x2 h0, h1;
      h0.x = 0.5f * (a1.x + a2.x); h0.y = 0.5f * (a1.y - a2.y);
      h1.x = 0.5f * (b1.x + b2.x); h1.y = 0.5f * (b1.y - b2.y);
      const f32x2 p0 = cmul(r0, h0), p1 = cmul(r1, h1);
      v[k2].x = p0.x - p1.y;
      v[k2].y = p0.y + p1.x;
    }
  }
  fft512_inv(v, buf, tw, lane);
  TS(3);
  wave_sync();
#pragma unroll
  for (int j = 0; j < 8; ++j) buf[lane + 64 * j] = v[j];
  SYNC();
  {
    f32x2 y[2][4];
#pragma unroll
    for (int k = 0; k < 4; ++k) {
      const f32x4 o = *(const f32x4*)(lds + k * PS + 2 * tid);
      y[0][k] = o.xy; y[1][k] = o.zw;
      if (k) { y[0][k] = cmulc(y[0][k], t4[0][k - 1]); y[1][k] = cmulc(y[1][k], t4[1][k - 1]); }
    }
    f32x2 o[2][4];
    dft4<1>(y[0][0], y[0][1], y[0][2], y[0][3], o[0][0], o[0][1], o[0][2], o[0][3]);
    dft4<1>(y[1][0], y[1][1], y[1][2], y[1][3], o[1][0], o[1][1], o[1][2], o[1][3]);
    const rsrc_t rw2 = mkrsrc(ws);
#pragma unroll
    for (int n3 = 0; n3 < 4; ++n3) {
      f32x4 w; w.xy = o[0][n3]; w.zw = o[1][n3];
      st16(rw2, (u32)WS_ZT + (u32)(kd * SEQ * 8 + (tid + 256 * n3) * 16), __builtin_bit_cast(u32x4, w));
    }
  }
  WAIT_V(0);
  SYNC();
  u32* sync = (u32*)(ws + WS_SYNC);
  if (tid == 0) __hip_atomic_fetch_add(sync + (44 + (kd & 15)) * 16, 1u, __ATOMIC_RELAXED, __HIP_MEMORY_SCOPE_AGENT);
  TS(4);

  const int s0 = kd * 8;
  const int row = s0 + 2 * wave;
  const float g0 = ((const float*)(ws + WS_G))[row], g1 = ((const float*)(ws + WS_G))[row + 1];
  if (wave == 0) {
    for (int it = 0; it < (1 << 22); ++it) {
      const u32 c = __hip_atomic_load(sync + (44 + (lane & 15)) * 16, __ATOMIC_RELAXED, __HIP_MEMORY_SCOPE_AGENT);
      if (__builtin_amdgcn_ballot_w64(c < 16) == 0) break;
      __builtin_amdgcn_s_sleep(8);
    }
  }
  SYNC();
  TS(5);
  const rsrc_t rws = mkrsrc(ws);
  u32x4 z[4];
#pragma unroll
  for (int r = 0; r < 4; ++r) {
    const int i = tid + 256 * r;
    z[r] = __builtin_amdgcn_raw_buffer_load_b128(rws, (u32)WS_ZT + (u32)((i >> 2) * SEQ + s0 + 2 * (i & 3)) * 8, 0, 16);
  }
#pragma unroll
  for (int r = 0; r < 4; ++r) {
    const int i = tid + 256 * r;
    const int kk = i >> 2, p = i & 3;
    const f32x4 qq = __builtin_bit_cast(f32x4, z[r]);
    f32x2 c1, c2;
    if (kk) {
      c1.x = qq.x - qq.w; c1.y = qq.y + qq.z;
      c2.x = qq.x + qq.w; c2.y = qq.z - qq.y;
    } else {
      c1.x = qq.x; c1.y = qq.z;
      c2.x = qq.y; c2.y = qq.w;
    }
    lds[p * PS3 + sslot(kk)] = c1;
    lds[p * PS3 + sslot(kk ? 512 - kk : 256)] = c2;
  }
  SYNC();
  TS(6);
  f32x2* buf3 = lds + wave * PS3;
#pragma unroll
  for (int k2 = 0; k2 < 8; ++k2) v[k2] = buf3[sslot(hi + 8 * lo + 64 * k2)];
  fft512_inv(v, buf3, tw, lane);
  wave_sync();
#pragma unroll
  for (int j = 0; j < 8; ++j) buf3[lane + 2 * (lane >> 3) + 80 * j] = v[j];
  wave_sync();
  f32x4 u[4];
#pragma unroll
  for (int j = 0; j < 4; ++j) u[j] = *(const f32x4*)(buf3 + 10 * lane + 2 * j);
  TS(7);
  for (int it = 0; it < (1 << 22); ++it) {
    if (__hip_atomic_load(sync + (8 + (s0 >> 6)) * 16, __ATOMIC_RELAXED, __HIP_MEMORY_SCOPE_AGENT) >= 8) break;
    __builtin_amdgcn_s_sleep(2);
  }
  asm volatile("" ::: "memory");
  TS(8);
  const u32x4 b0 = __builtin_amdgcn_raw_buffer_load_b128(rws, (u32)WS_B + (u32)(row * DM + lane * 8) * 2, 0, 16);
  const u32x4 b1 = __builtin_amdgcn_raw_buffer_load_b128(rws, (u32)WS_B + (u32)((row + 1) * DM + lane * 8) * 2, 0, 16);
  const float sc = 1.0f / (2048.0f * 512.0f);
  const float og0 = bfr(1.0f - g0), og1 = bfr(1.0f - g1);
  u32x4 o0, o1;
#pragma unroll
  for (int j = 0; j < 4; ++j) {
    const f32x2 bv0 = unpk(b0[j]), bv1 = unpk(b1[j]);
    o0[j] = pack2(bfr(g0 * bfr(u[j].x * sc)) + bfr(og0 * bv0.x), bfr(g0 * bfr(u[j].z * sc)) + bfr(og0 * bv0.y));
    o1[j] = pack2(bfr(g1 * bfr(u[j].y * sc)) + bfr(og1 * bv1.x), bfr(g1 * bfr(u[j].w * sc)) + bfr(og1 * bv1.y));
  }
  const rsrc_t ro = mkrsrc(out);
  st16(ro, (u32)(row * DM + lane * 8) * 2, o0);
  st16(ro, (u32)((row + 1) * DM + lane * 8) * 2, o1);
  TS(9);
}

NOPK __global__ __launch_bounds__(256, 2) void k_l2(char* __restrict__ ws, u16* __restrict__ out) {
  extern __shared__ __attribute__((aligned(16))) char smem[];
  if (BIDX >= 256) g2_body(ws, smem, BIDX - 256);
  else k2_body(ws, smem, BIDX, out);
}

extern "C" void kernel_launch(void* const* d_in, const int* in_sizes, int n_in, void* d_out, int out_size,
                              void* d_ws, size_t ws_size, hipStream_t stream) {
  const u16* x = (const u16*)d_in[0];
  const u16* wr = (const u16*)d_in[1];
  const u16* wi = (const u16*)d_in[2];
  const u16* wg = (const u16*)d_in[4];
  const u16* wu = (const u16*)d_in[5];
  const u16* wd = (const u16*)d_in[6];
  const u16* gw = (const u16*)d_in[7];
  char* ws = (char*)d_ws;
  (void)hipFuncSetAttribute((const void*)k_l1, hipFuncAttributeMaxDynamicSharedMemorySize, L1_LDS);
  (void)hipFuncSetAttribute((const void*)k_l2, hipFuncAttributeMaxDynamicSharedMemorySize, L2_LDS);
  k_l1<<<256, 1024, L1_LDS, stream>>>(x, wr, wi, wg, wu, wd, gw, ws);
  k_l2<<<512, 256, L2_LDS, stream>>>(ws, (u16*)d_out);
}
```

```cpp
#include <hip/hip_runtime.h>
#include <hip/hip_bf16.h>
#include <stdint.h>

#if defined(__HIP_DEVICE_COMPILE__)
#define NOPK __attribute__((target("no-packed-fp32-ops")))
#else
#define NOPK
#endif
#define DEVI NOPK __device__ __forceinline__

typedef __attribute__((ext_vector_type(8))) short bf16x8;
typedef __attribute__((ext_vector_type(4))) short bf16x4;
typedef __attribute__((ext_vector_type(4))) float f32x4;
typedef __attribute__((ext_vector_type(2))) float f32x2;
typedef unsigned short u16;
typedef unsigned int u32;
typedef __attribute__((ext_vector_type(4))) unsigned int u32x4;
typedef __attribute__((ext_vector_type(2))) unsigned int u32x2;
typedef __attribute__((address_space(3))) bf16x4* lds_b4_ptr;
#define TIDX ((int)__builtin_amdgcn_workitem_id_x())
#define BIDX ((int)__builtin_amdgcn_workgroup_id_x())
#define U2F(x) __builtin_bit_cast(float, (u32)(x))
#define SYNC() do { __builtin_amdgcn_fence(__ATOMIC_RELEASE, "workgroup"); __builtin_amdgcn_s_barrier(); \
                    __builtin_amdgcn_fence(__ATOMIC_ACQUIRE, "workgroup"); } while (0)
DEVI float fexp(float x) { return __builtin_amdgcn_exp2f(x * 1.44269504088896340736f); }
DEVI float shfl_xor(float v, int m, int lane) {
  return __builtin_bit_cast(float, __builtin_amdgcn_ds_bpermute((lane ^ m) << 2, __builtin_bit_cast(int, v)));
}

constexpr int SEQ = 2048, DM = 512, DF = 257, FF = 1376;
constexpr int KS32 = FF / 32;

constexpr size_t WS_YT = 0;
constexpr size_t WS_WT = WS_YT + (size_t)DF * SEQ * 8;
constexpr size_t WS_H = WS_WT + (size_t)DF * SEQ * 4;
constexpr size_t WS_WD = WS_H + (size_t)SEQ * FF * 2;
constexpr size_t WS_B = WS_WD + (size_t)FF * DM * 2;
constexpr size_t WS_G = WS_B + (size_t)SEQ * DM * 2;
constexpr size_t WS_SYNC = WS_G + 8192;
constexpr size_t WS_ZT = WS_SYNC + 4096;

DEVI float bf2f(u16 v) { return U2F(((u32)v) << 16); }
DEVI u16 f2bf(float f) {
  __bf16 b = (__bf16)f;
  return __builtin_bit_cast(u16, b);
}
DEVI float bfr(float f) { return bf2f(f2bf(f)); }

DEVI f32x2 cmul(f32x2 a, f32x2 b) {
  f32x2 r;
  r.x = a.x * b.x - a.y * b.y;
  r.y = a.x * b.y + a.y * b.x;
  return r;
}
DEVI f32x2 cmulc(f32x2 a, f32x2 b) {
  f32x2 r;
  r.x = a.x * b.x + a.y * b.y;
  r.y = a.y * b.x - a.x * b.y;
  return r;
}
DEVI f32x2 twid(float frac) {
  f32x2 r;
  r.x = __builtin_amdgcn_cosf(frac);
  r.y = -__builtin_amdgcn_sinf(frac);
  return r;
}

template <int SGN>
DEVI f32x2 mul_i(f32x2 z) {
  f32x2 r;
  if (SGN < 0) { r.x = z.y; r.y = -z.x; } else { r.x = -z.y; r.y = z.x; }
  return r;
}
template <int SGN>
DEVI void dft4(f32x2 b0, f32x2 b1, f32x2 b2, f32x2 b3, f32x2& y0, f32x2& y1, f32x2& y2,
                                     f32x2& y3) {
  f32x2 c0 = b0 + b2, c1 = b0 - b2, c2 = b1 + b3, c3 = mul_i<SGN>(b1 - b3);
  y0 = c0 + c2; y2 = c0 - c2; y1 = c1 + c3; y3 = c1 - c3;
}
template <int SGN>
DEVI void dft8(f32x2 (&v)[8]) {
  const float R = 0.70710678118654752440f;
  f32x2 a0 = v[0] + v[4], a4 = v[0] - v[4];
  f32x2 a1 = v[1] + v[5], a5 = v[1] - v[5];
  f32x2 a2 = v[2] + v[6], a6 = v[2] - v[6];
  f32x2 a3 = v[3] + v[7], a7 = v[3] - v[7];
  f32x2 t5, t7;
  if (SGN < 0) {
    t5.x = (a5.x + a5.y) * R; t5.y = (a5.y - a5.x) * R;
    t7.x = (a7.y - a7.x) * R; t7.y = -(a7.x + a7.y) * R;
  } else {
    t5.x = (a5.x - a5.y) * R; t5.y = (a5.x + a5.y) * R;
    t7.x = -(a7.x + a7.y) * R; t7.y = (a7.x - a7.y) * R;
  }
  f32x2 t6 = mul_i<SGN>(a6);
  dft4<SGN>(a0, a1, a2, a3, v[0], v[2], v[4], v[6]);
  dft4<SGN>(a4, t5, t6, t7, v[1], v[3], v[5], v[7]);
}

DEVI void wave_sync() {
  asm volatile("s_waitcnt lgkmcnt(0)" ::: "memory");
  __builtin_amdgcn_wave_barrier();
}

struct Tw512 {
  f32x2 a[7];
  f32x2 b[7];
};
DEVI void make_tw512(Tw512& t, int lane) {
#pragma unroll
  for (int k = 1; k < 8; ++k) {
    t.a[k - 1] = twid((float)((lane * k) & 511) * (1.0f / 512.0f));
    t.b[k - 1] = twid((float)(((lane & 7) * k) & 63) * (1.0f / 64.0f));
  }

}

DEVI void fft512_fwd(f32x2 (&v)[8], f32x2* buf, const Tw512& tw, int lane) {
  const int hi = lane >> 3, lo = lane & 7;
  dft8<-1>(v);
#pragma unroll
  for (int k = 1; k < 8; ++k) v[k] = cmul(v[k], tw.a[k - 1]);
  wave_sync();
#pragma unroll
  for (int k = 0; k < 8; ++k) buf[72 * k + lane] = v[k];
  wave_sync();
#pragma unroll
  for (int n = 0; n < 8; ++n) v[n] = buf[72 * hi + 8 * n + lo];
  dft8<-1>(v);
#pragma unroll
  for (int k = 1; k < 8; ++k) v[k] = cmul(v[k], tw.b[k - 1]);
  wave_sync();
#pragma unroll
  for (int k = 0; k < 8; ++k) buf[72 * hi + 9 * k + lo] = v[k];
  wave_sync();
#pragma unroll
  for (int n = 0; n < 8; ++n) v[n] = buf[72 * hi + 9 * lo + n];
  dft8<-1>(v);
}
DEVI void fft512_inv(f32x2 (&v)[8], f32x2* buf, const Tw512& tw, int lane) {
  const int hi = lane >> 3, lo = lane & 7;
  dft8<1>(v);
  wave_sync();
#pragma unroll
  for (int n = 0; n < 8; ++n) buf[72 * hi + 9 * lo + n] = v[n];
  wave_sync();
#pragma unroll
  for (int k = 0; k < 8; ++k) v[k] = buf[72 * hi + 9 * k + lo];
#pragma unroll
  for (int k = 1; k < 8; ++k) v[k] = cmulc(v[k], tw.b[k - 1]);
  dft8<1>(v);
  wave_sync();
#pragma unroll
  for (int n = 0; n < 8; ++n) buf[72 * hi + 8 * n + lo] = v[n];
  wave_sync();
#pragma unroll
  for (int k = 0; k < 8; ++k) v[k] = buf[72 * k + lane];
#pragma unroll
  for (int k = 1; k < 8; ++k) v[k] = cmulc(v[k], tw.a[k - 1]);
  dft8<1>(v);
}

DEVI int sslot(int k) { return k + 4 * (k >> 5); }
constexpr int PS = 584;

#define AUX_GATE 7
constexpr int G1_NS = 3;
constexpr int G1_X_OFF = G1_NS * 24576;
constexpr int AX_OFF = G1_X_OFF + G1_NS * 16384;
constexpr int AX_W_OFF = AX_OFF + 4 * PS * 8;
constexpr int AX_WD_OFF = AX_W_OFF + 10240;
constexpr int AX_GW_OFF = AX_WD_OFF;
constexpr int L1_CNT_OFF = AX_WD_OFF + 8192;
constexpr int L1_LDS = L1_CNT_OFF + 256;

#define WAIT_V(n) asm volatile("s_waitcnt vmcnt(" #n ")" ::: "memory")
#define WAIT_L(n) asm volatile("s_waitcnt lgkmcnt(" #n ")" ::: "memory")
#define RAW_BAR() asm volatile("s_barrier" ::: "memory")

typedef __amdgpu_buffer_rsrc_t rsrc_t;
typedef __attribute__((ext_vector_type(4))) int i32x4;
DEVI void dma16(const void* gsrc, u32 lds_dst) {
  u32 keep;
  asm volatile("s_mov_b32 %0, m0\n\ts_mov_b32 m0, %2\n\ts_nop 0\n\tglobal_load_lds_dwordx4 %1, off\n\ts_mov_b32 m0, %0"
               : "=&s"(keep) : "v"(gsrc), "s"(lds_dst) : "memory");
}
DEVI void dma16b(i32x4 rs, u32 voff, u32 soff, u32 lds_dst) {
  u32 keep;
  asm volatile("s_mov_b32 %0, m0\n\ts_mov_b32 m0, %4\n\ts_nop 0\n\tbuffer_load_dwordx4 %1, %2, %3 offen lds\n\ts_mov_b32 m0, %0"
               : "=&s"(keep) : "v"(voff), "s"(rs), "s"(soff), "s"(lds_dst) : "memory");
}
DEVI i32x4 mkrs(const void* p, u32 bytes) {
  const unsigned long long a = (unsigned long long)p;
  i32x4 r;
  r.x = __builtin_amdgcn_readfirstlane((int)(u32)a);
  r.y = __builtin_amdgcn_readfirstlane((int)(u32)(a >> 32) & 0xffff);
  r.z = (int)bytes;
  r.w = 0x00020000;
  return r;
}
DEVI rsrc_t mkrsrc(const void* p) { return __builtin_amdgcn_make_buffer_rsrc((void*)p, 0, 0x7fffffff, 0x00020000); }
DEVI void st16(rsrc_t r, u32 off, u32x4 v) { __builtin_amdgcn_raw_buffer_store_b128(v, r, off, 0, 16); }
DEVI u32 pack2(float a, float b) { return (u32)f2bf(a) | ((u32)f2bf(b) << 16); }

typedef __attribute__((address_space(3))) u32* lds_u32_ptr;
DEVI void lds_signal(u32 addr, int lane) {
  unsigned long long keep;
  asm volatile("s_mov_b64 %0, exec\n\ts_mov_b64 exec, 1\n\tds_add_u32 %1, %2\n\ts_mov_b64 exec, %0"
               : "=&s"(keep) : "v"(addr), "v"(1u) : "memory");
}
DEVI void lds_wait1(u32 addr, u32 need) {
  while (true) {
    u32 v;
    asm volatile("ds_read_b32 %0, %1\n\ts_waitcnt lgkmcnt(0)" : "=v"(v) : "v"(addr) : "memory");
    if ((u32)__builtin_amdgcn_readfirstlane(v) >= need) break;
    __builtin_amdgcn_s_sleep(1);
  }
}
DEVI void aux_body(const u16* __restrict__ x, const u16* __restrict__ wr, const u16* __restrict__ wi,
                   const u16* __restrict__ wd, const u16* __restrict__ gw, char* __restrict__ ws, char* smem, int a,
                   int wave, int lane, bool gated) {
  const u32 lds0 = (u32)(size_t)(__attribute__((address_space(3))) char*)smem;
  const rsrc_t rws = mkrsrc(ws);
  const int s0 = a * 8;
  const int tid = wave * 64 + lane;
  f32x2* aux = (f32x2*)(smem + AX_OFF);
  {
    const int p = wave;
    Tw512 tw;
    make_tw512(tw, lane);
    WAIT_V(0);
    const u16* xa = (const u16*)(aux + p * PS);
    const u16* gl = (const u16*)(smem + AX_GW_OFF + p * 1024);
    u16 ra[8], rb[8], rg[8];
#pragma unroll
    for (int j = 0; j < 8; ++j) {
      ra[j] = xa[lane + 64 * j];
      rb[j] = xa[DM + lane + 64 * j];
      rg[j] = gl[lane + 64 * j];
    }
    f32x2 v[8];
    float sa = 0.f, sb = 0.f;
#pragma unroll
    for (int j = 0; j < 8; ++j) {
      v[j].x = bf2f(ra[j]);
      v[j].y = bf2f(rb[j]);
      float w = bf2f(rg[j]);
      sa += bfr(v[j].x * w);
      sb += bfr(v[j].y * w);
    }
#pragma unroll
    for (int o = 32; o > 0; o >>= 1) {
      sa += shfl_xor(sa, o, lane);
      sb += shfl_xor(sb, o, lane);
    }
    if (lane < 2) {
      float s = bfr(lane ? sb : sa);
      float gg = bfr(1.0f / (1.0f + fexp(-s)));
      ((float*)(ws + WS_G))[s0 + 2 * p + lane] = gg;
    }
    f32x2* buf = aux + p * PS;
    fft512_fwd(v, buf, tw, lane);
    wave_sync();
    const int hi = lane >> 3, lo = lane & 7;
#pragma unroll
    for (int k2 = 0; k2 < 8; ++k2) buf[sslot(hi + 8 * lo + 64 * k2)] = v[k2];
  }
  WAIT_L(0);
  lds_signal(lds0 + L1_CNT_OFF + 42 * 4, lane);
  if (gated) lds_wait1(lds0 + L1_CNT_OFF + 44 * 4, 4);
  {
    const int p = wave;
    const char* pr = (const char*)wr + (size_t)a * 4112;
    const char* pi = (const char*)wi + (size_t)a * 4112;
    dma16(pr + p * 1024 + lane * 16, lds0 + AX_W_OFF + p * 1024);
    dma16(pi + p * 1024 + lane * 16, lds0 + AX_W_OFF + 5120 + p * 1024);
    if (p < 2) dma16((p ? pi : pr) + (lane == 0 ? 4096 : 0), lds0 + AX_W_OFF + p * 5120 + 4096);
    const char* pd = (const char*)wd + (size_t)(a < 172 ? a : 0) * 8192;
    dma16(pd + p * 1024 + lane * 16, lds0 + AX_WD_OFF + p * 1024);
    dma16(pd + 4096 + p * 1024 + lane * 16, lds0 + AX_WD_OFF + 4096 + p * 1024);
  }
  lds_wait1(lds0 + L1_CNT_OFF + 42 * 4, 4);
#pragma unroll
  for (int r = 0; r < 4; ++r) {
    const int i = tid + 256 * r;
    const int kd = i >> 2, p = i & 3;
    const f32x2 z1 = aux[p * PS + sslot(kd)];
    const f32x2 z2 = aux[p * PS + sslot(kd ? 512 - kd : 256)];
    f32x4 o;
    if (kd) {
      o.x = 0.5f * (z1.x + z2.x);
      o.y = 0.5f * (z1.y - z2.y);
      o.z = 0.5f * (z1.y + z2.y);
      o.w = 0.5f * (z2.x - z1.x);
    } else {
      o.x = z1.x; o.y = z2.x; o.z = z1.y; o.w = z2.y;
    }
    st16(rws, (u32)WS_YT + (u32)(kd * SEQ + s0 + 2 * p) * 8, __builtin_bit_cast(u32x4, o));
  }
  WAIT_V(4);
  lds_signal(lds0 + L1_CNT_OFF + 43 * 4, lane);
  lds_wait1(lds0 + L1_CNT_OFF + 43 * 4, 4);
  {
    const u16* lr = (const u16*)(smem + AX_W_OFF);
    const u16* li = (const u16*)(smem + AX_W_OFF + 5120);
#pragma unroll
    for (int r = 0; r < 3; ++r) {
      const int i = tid + 256 * r;
      if (i < DF * 2) {
        const int kd = i >> 1, h = i & 1;
        u32x4 o;
#pragma unroll
        for (int q = 0; q < 4; ++q) o[q] = (u32)lr[(h * 4 + q) * DF + kd] | ((u32)li[(h * 4 + q) * DF + kd] << 16);
        st16(rws, (u32)WS_WT + (u32)(kd * SEQ + s0 + h * 4) * 4, o);
      }
    }
  }
  if (a < 172) {
    const u16* ld = (const u16*)(smem + AX_WD_OFF);
#pragma unroll
    for (int r = 0; r < 2; ++r) {
      const int n = tid + 256 * r;
      u32x4 o;
#pragma unroll
      for (int q = 0; q < 4; ++q) o[q] = (u32)ld[(2 * q) * DM + n] | ((u32)ld[(2 * q + 1) * DM + n] << 16);
      const int ks = a >> 2, kq = a & 3;
      st16(rws, (u32)WS_WD + (u32)(((ks * 32 + (n >> 4)) * 64 + (n & 15) + 16 * kq) * 16), o);
    }
  }
}

NOPK __global__ __launch_bounds__(1024, 4) void k_l1(const u16* __restrict__ x, const u16* __restrict__ wr,
                                                const u16* __restrict__ wi, const u16* __restrict__ wg,
                                                const u16* __restrict__ wu, const u16* __restrict__ wd,
                                                const u16* __restrict__ gw, char* __restrict__ ws) {
  extern __shared__ __attribute__((aligned(16))) char smem[];
  const int bid = BIDX;
  const int tid = TIDX, lane = tid & 63;
  const int wave = __builtin_amdgcn_readfirstlane(tid >> 6);
  const u32 lds0 = (u32)(size_t)(__attribute__((address_space(3))) char*)smem;
  const u32 cnt = lds0 + L1_CNT_OFF;
  if (wave == 0) { ((u32*)(smem + L1_CNT_OFF))[lane] = 0; WAIT_L(0); }
  RAW_BAR();
  const int xcd = bid & 7, idx = bid >> 3;
  const int T = xcd * 30 + (idx < 30 ? idx : 0);
  const int fg = T / 80, rem = T - fg * 80;
  const int mt_ = rem / 5, fi = rem - mt_ * 5;
  const int m0 = mt_ * 128, f0 = (fg * 5 + fi) * 96;
  if (wave >= 12) {
    __builtin_amdgcn_s_setprio(2);
    const int w = wave - 12;
    if (bid == 255 && w == 0) ((u32*)(ws + WS_SYNC))[lane * 16] = 0;
    const char* px = (const char*)x + (size_t)(bid * 8 + 2 * w) * 1024;
    dma16(px + lane * 16, lds0 + AX_OFF + w * (PS * 8));
    dma16(px + 1024 + lane * 16, lds0 + AX_OFF + w * (PS * 8) + 1024);
    dma16((const char*)gw + lane * 16, lds0 + AX_GW_OFF + w * 1024);
    aux_body(x, wr, wi, wd, gw, ws, smem, bid, w, lane, idx < 30);
    return;
  }
  if (idx >= 30) return;

  {
    const int q4 = wave >= 8 ? wave - 8 : (wave >> 1);
    const i32x4 rx = mkrs(x, SEQ * DM * 2), rg = mkrs(wg, DM * FF * 2), ru = mkrs(wu, DM * FF * 2);
    u32 vx, vw[3];
    {
      const int row = lane >> 3;
      const int c = (lane & 7) ^ row;
      vx = (u32)((m0 + q4 * 32 + row) * DM + c * 8) * 2;
#pragma unroll
      for (int j = 0; j < 3; ++j) {
        const int q = 64 * j + lane;
        const int k = q / 12, cp = q - k * 12;
        const int c2 = cp ^ (((k >> 3) & 1) << 1);
        vw[j] = (u32)((q4 * 16 + k) * FF + f0 + c2 * 8) * 2;
      }
    }
    auto issue = [&](int s, int lo, int hi) NOPK __attribute__((always_inline)) {
      const int slot = s % G1_NS;
      const u32 wdst = lds0 + slot * 24576 + q4 * 3072, xdst = lds0 + G1_X_OFF + slot * 16384 + q4 * 4096;
      const u32 sw = s * (64 * FF * 2);
#pragma unroll
      for (int n = 0; n < 10; ++n) {
        if (n >= lo && n < hi) {
          if (n < 3) dma16b(rg, vw[n], sw, wdst + n * 1024);
          else if (n < 7) dma16b(rx, vx, s * 128 + (n - 3) * 8192, xdst + (n - 3) * 1024);
          else dma16b(ru, vw[n - 7], sw, wdst + 12288 + (n - 7) * 1024);
        }
      }
    };
    if (wave >= 8) {
      issue(1, 0, 10);
      issue(2, 0, 5);
      WAIT_V(5);
      lds_signal(cnt + 1 * 4, lane);
      issue(2, 5, 10);
#pragma unroll 1
      for (int s = 3; s < 8; ++s) {
        lds_wait1(cnt + (16 + s - 3) * 4, 8);
        issue(s, 0, 3);
        WAIT_V(3);
        lds_signal(cnt + (s - 1) * 4, lane);
        issue(s, 3, 10);
        if (s == AUX_GATE) lds_signal(cnt + 44 * 4, lane);
      }
      WAIT_V(0);
      lds_signal(cnt + 7 * 4, lane);
      return;
    }
    if (wave & 1) issue(0, 5, 10); else issue(0, 0, 5);
    WAIT_V(0);
    lds_signal(cnt, lane);
  }

  const int wm = wave >> 1, wf = wave & 1;
  const int l15 = lane & 15, lq = lane >> 4;
  int xoff[2][2];
#pragma unroll
  for (int mt = 0; mt < 2; ++mt)
#pragma unroll
    for (int kk = 0; kk < 2; ++kk) {
      const int row = wm * 32 + mt * 16 + l15;
      xoff[mt][kk] = G1_X_OFF + row * 128 + (((kk * 4 + lq) ^ (row & 7)) << 4);
    }
  const int wq = l15 >> 2, wp = l15 & 3;
  int woff[3];
#pragma unroll
  for (int ft = 0; ft < 3; ++ft)
    woff[ft] = (8 * lq + wq) * 192 + (((2 * (wf * 3 + ft) + (wp >> 1)) ^ (2 * (lq & 1))) << 4) + (wp & 1) * 8;

  f32x4 accg[3][2], accu[3][2];
#pragma unroll
  for (int a = 0; a < 3; ++a)
#pragma unroll
    for (int b = 0; b < 2; ++b) { accg[a][b] = (f32x4)0.f; accu[a][b] = (f32x4)0.f; }

  bf16x8 xa[2], xb[2], wgf[3], wuf[3];
  auto rdx = [&](bf16x8(&xf)[2], int xs, int kk) NOPK __attribute__((always_inline)) {
#pragma unroll
    for (int mt = 0; mt < 2; ++mt) xf[mt] = *(const bf16x8*)(smem + xs + xoff[mt][kk]);
  };
  auto rdw = [&](bf16x8(&wfr)[3], int wsl, int kk, int mat) NOPK __attribute__((always_inline)) {
#pragma unroll
    for (int n = 0; n < 3; ++n) {
      const char* base = smem + (wsl + woff[n]) + kk * 6144 + mat * 12288;
      bf16x4 lo = __builtin_amdgcn_ds_read_tr16_b64_v4i16((lds_b4_ptr)(base));
      bf16x4 hi = __builtin_amdgcn_ds_read_tr16_b64_v4i16((lds_b4_ptr)(base + 768));
      wfr[n] = __builtin_shufflevector(lo, hi, 0, 1, 2, 3, 4, 5, 6, 7);
    }
  };
  auto mma = [&](f32x4(&acc)[3][2], bf16x8(&xf)[2], bf16x8(&wfr)[3]) NOPK __attribute__((always_inline)) {
#pragma unroll
    for (int ft = 0; ft < 3; ++ft)
#pragma unroll
      for (int mt = 0; mt < 2; ++mt)
        acc[ft][mt] = __builtin_amdgcn_mfma_f32_16x16x32_bf16(wfr[ft], xf[mt], acc[ft][mt], 0, 0, 0);
  };
  auto interleave = [&]() NOPK __attribute__((always_inline)) {
#pragma unroll
    for (int i = 0; i < 6; ++i) {
      __builtin_amdgcn_sched_group_barrier(0x008, 1, 0);
      __builtin_amdgcn_sched_group_barrier(0x100, 2, 0);
    }
  };
  const volatile u32* cv = (const volatile u32*)(smem + L1_CNT_OFF);

  lds_wait1(cnt, 8);
  rdx(xa, 0, 0);
  rdw(wgf, 0, 0, 0);
#define SB() __builtin_amdgcn_sched_barrier(0)
  int slot = 0;
#pragma unroll 1
  for (int S = 0; S < 8; ++S) {
    const int wsl = slot * 24576, xs = slot * 16384;
    slot = slot == G1_NS - 1 ? 0 : slot + 1;
    const int wsn = slot * 24576, xn = slot * 16384;
    SB(); rdw(wuf, wsl, 0, 1); mma(accg, xa, wgf); interleave();
    SB(); rdw(wgf, wsl, 1, 0); rdx(xb, xs, 1); mma(accu, xa, wuf); interleave();
    SB(); rdw(wuf, wsl, 1, 1); const u32 fv = cv[S + 1]; mma(accg, xb, wgf); interleave();
    SB();
    WAIT_L(0);
    lds_signal(cnt + (16 + S) * 4, lane);
    if (S < 7) {
      if ((u32)__builtin_amdgcn_readfirstlane(fv) < 4) lds_wait1(cnt + (S + 1) * 4, 4);
    }
    SB();
    rdw(wgf, wsn, 0, 0); rdx(xa, xn, 0);
    mma(accu, xb, wuf);
    interleave();
  }
  SB();
  const rsrc_t rws = mkrsrc(ws);
  const int lane2 = __builtin_amdgcn_mbcnt_hi(~0u, __builtin_amdgcn_mbcnt_lo(~0u, 0u));
  const int e15 = lane2 & 15, eq = lane2 >> 4;
#pragma unroll
  for (int ft = 0; ft < 3; ++ft) {
    u32 pk[2][2];
#pragma unroll
    for (int mt = 0; mt < 2; ++mt) {
      float o[4];
#pragma unroll
      for (int r = 0; r < 4; ++r) {
        const float g = bfr(accg[ft][mt][r]);
        const float sl = bfr(g * __builtin_amdgcn_rcpf(1.0f + fexp(-g)));
        const float u = bfr(accu[ft][mt][r]);
        o[r] = sl * u;
      }
      pk[mt][0] = pack2(o[0], o[1]);
      pk[mt][1] = pack2(o[2], o[3]);
    }
    auto s0_ = __builtin_amdgcn_permlane16_swap(pk[0][0], pk[1][0], false, false);
    auto s1_ = __builtin_amdgcn_permlane16_swap(pk[0][1], pk[1][1], false, false);
    u32x4 ov;
    ov.x = s0_[0]; ov.y = s1_[0]; ov.z = s0_[1]; ov.w = s1_[1];
    const int f = f0 + (wf * 3 + ft) * 16 + 8 * (eq >> 1);
    const int m = m0 + wm * 32 + (eq & 1) * 16 + e15;
    if (f < FF)
      st16(rws, (u32)WS_H + (u32)(((m >> 4) * KS32 + (f >> 5)) * 1024 + ((m & 15) + 16 * ((f & 31) >> 3)) * 16), ov);
  }
}

constexpr int L2_LDS = 4 * PS * 8 + 32768;
constexpr int G2_DEPTH = 4;

DEVI void g2_body(char* __restrict__ ws, char* smem, int bid) {
  const int tid = TIDX, lane = tid & 63;
  const int wave = __builtin_amdgcn_readfirstlane(tid >> 6);
  const int xcd = bid & 7, idx = bid >> 3;
  const int mt64 = xcd * 4 + (idx >> 3), nt64 = idx & 7;
  const char* hb = ws + WS_H + (size_t)(mt64 * 4) * KS32 * 1024 + lane * 16;
  const char* wb = ws + WS_WD + (size_t)(nt64 * 4) * 1024 + lane * 16;
  f32x4 acc[4][4];
#pragma unroll
  for (int a = 0; a < 4; ++a)
#pragma unroll
    for (int b = 0; b < 4; ++b) acc[a][b] = (f32x4)0.f;
  constexpr int NS = G2_DEPTH + 1;
  bf16x8 hf[NS][4], wf[NS][4];
  auto ld = [&](int slot, int i) NOPK __attribute__((always_inline)) {
    int ks = wave + 4 * i;
    ks = ks > KS32 - 1 ? KS32 - 1 : ks;
#pragma unroll
    for (int j = 0; j < 4; ++j) {
      hf[slot][j] = *(const bf16x8*)(hb + ((size_t)j * KS32 + ks) * 1024);
      wf[slot][j] = *(const bf16x8*)(wb + ((size_t)ks * 32 + j) * 1024);
    }
  };
#pragma unroll
  for (int i = 0; i < G2_DEPTH; ++i) ld(i, i);
  __builtin_amdgcn_sched_barrier(0);
#pragma unroll
  for (int i = 0; i < 11; ++i) {
    if (i + G2_DEPTH < 11) ld((i + G2_DEPTH) % NS, i + G2_DEPTH);
    __builtin_amdgcn_sched_barrier(0);
    if (i < 10 || wave < 3) {
#pragma unroll
      for (int nt = 0; nt < 4; ++nt)
#pragma unroll
        for (int mt = 0; mt < 4; ++mt)
          acc[nt][mt] = __builtin_amdgcn_mfma_f32_16x16x32_bf16(wf[i % NS][nt], hf[i % NS][mt], acc[nt][mt], 0, 0, 0);
    }
    __builtin_amdgcn_sched_barrier(0);
  }
  f32x4* red = (f32x4*)smem;
#pragma unroll
  for (int nt = 0; nt < 4; ++nt) {
    if (nt != wave) {
      const int src = wave < nt ? wave : wave - 1;
#pragma unroll
      for (int mt = 0; mt < 4; ++mt) red[((nt * 3 + src) * 4 + mt) * 64 + lane] = acc[nt][mt];
    }
  }
  SYNC();
  f32x4 fin[4];
#pragma unroll
  for (int nt = 0; nt < 4; ++nt)
    if (nt == wave) {
#pragma unroll
      for (int mt = 0; mt < 4; ++mt) fin[mt] = acc[nt][mt];
    }
  const rsrc_t rws = mkrsrc(ws);
  const int l15 = lane & 15, lq = lane >> 4;
#pragma unroll
  for (int mt = 0; mt < 4; ++mt)
#pragma unroll
    for (int s = 0; s < 3; ++s) fin[mt] += red[((wave * 3 + s) * 4 + mt) * 64 + lane];
#pragma unroll
  for (int pr = 0; pr < 2; ++pr) {
    auto s0_ = __builtin_amdgcn_permlane16_swap(pack2(fin[2 * pr][0], fin[2 * pr][1]),
                                                pack2(fin[2 * pr + 1][0], fin[2 * pr + 1][1]), false, false);
    auto s1_ = __builtin_amdgcn_permlane16_swap(pack2(fin[2 * pr][2], fin[2 * pr][3]),
                                                pack2(fin[2 * pr + 1][2], fin[2 * pr + 1][3]), false, false);
    u32x4 ov;
    ov.x = s0_[0]; ov.y = s1_[0]; ov.z = s0_[1]; ov.w = s1_[1];
    const int m = mt64 * 64 + (2 * pr + (lq & 1)) * 16 + l15;
    const int n = nt64 * 64 + wave * 16 + 8 * (lq >> 1);
    st16(rws, (u32)WS_B + (u32)(m * DM + n) * 2, ov);
  }
  WAIT_V(0);
  SYNC();
  if (tid == 0) __hip_atomic_fetch_add((u32*)(ws + WS_SYNC) + (8 + mt64) * 16, 1u, __ATOMIC_RELAXED, __HIP_MEMORY_SCOPE_AGENT);
}

DEVI f32x2 unpk(u32 w) {
  f32x2 r;
  r.x = U2F(w << 16);
  r.y = U2F(w & 0xffff0000u);
  return r;
}

constexpr int PS3 = 648;
DEVI void k2_body(char* __restrict__ ws, char* smem, int kd, u16* __restrict__ out) {
  const int tid = TIDX, lane = tid & 63;
  const int wave = __builtin_amdgcn_readfirstlane(tid >> 6);
  const f32x4* col = (const f32x4*)(ws + WS_YT + (size_t)kd * SEQ * 8);
  const u32* wt = (const u32*)(ws + WS_WT) + (size_t)kd * SEQ;
  f32x2* lds = (f32x2*)smem;
  f32x4 q[4];
#pragma unroll
  for (int n3 = 0; n3 < 4; ++n3) q[n3] = col[tid + 256 * n3];
  const int hi = lane >> 3, lo = lane & 7;
  const int kbase = wave + 4 * (hi + 8 * lo);
  u32 wv[8];
#pragma unroll
  for (int k2 = 0; k2 < 8; ++k2) wv[k2] = wt[kbase + 256 * k2];
  u32 wx[8];
  if (kd == 0) {
    const u32* wn = (const u32*)(ws + WS_WT) + (size_t)256 * SEQ;
#pragma unroll
    for (int k2 = 0; k2 < 8; ++k2) wx[k2] = wn[kbase + 256 * k2];
  }
  Tw512 tw;
  make_tw512(tw, lane);
  f32x2 t4[2][3];
#pragma unroll
  for (int h = 0; h < 2; ++h)
#pragma unroll
    for (int k = 1; k < 4; ++k) t4[h][k - 1] = twid((float)(((2 * tid + h) * k) & 2047) * (1.0f / 2048.0f));
  {
    f32x2 y[2][4];
    dft4<-1>(q[0].xy, q[1].xy, q[2].xy, q[3].xy, y[0][0], y[0][1], y[0][2], y[0][3]);
    dft4<-1>(q[0].zw, q[1].zw, q[2].zw, q[3].zw, y[1][0], y[1][1], y[1][2], y[1][3]);
#pragma unroll
    for (int k = 0; k < 4; ++k) {
      f32x2 a = y[0][k], b = y[1][k];
      if (k) { a = cmul(a, t4[0][k - 1]); b = cmul(b, t4[1][k - 1]); }
      f32x4 o; o.xy = a; o.zw = b;
      *(f32x4*)(lds + k * PS + 2 * tid) = o;
    }
  }
  SYNC();
  f32x2* buf = lds + wave * PS;
  f32x2 v[8];
#pragma unroll
  for (int j = 0; j < 8; ++j) v[j] = buf[lane + 64 * j];
  fft512_fwd(v, buf, tw, lane);
  if (kd != 0) {
#pragma unroll
    for (int k2 = 0; k2 < 8; ++k2) v[k2] = cmul(v[k2], unpk(wv[k2]));
  } else {
    f32x4* ex = (f32x4*)(lds + 4 * PS);
#pragma unroll
    for (int k2 = 0; k2 < 8; ++k2) {
      f32x4 e; e.xy = v[k2]; e.z = U2F(wv[k2]); e.w = U2F(wx[k2]);
      ex[kbase + 256 * k2] = e;
    }
    SYNC();
#pragma unroll
    for (int k2 = 0; k2 < 8; ++k2) {
      const int k = kbase + 256 * k2, kn = (2048 - k) & 2047;
      const f32x4 e = ex[kn];
      const f32x2 c1 = v[k2], c2 = e.xy;
      f32x2 r0, r1;
      r0.x = 0.5f * (c1.x + c2.x); r0.y = 0.5f * (c1.y - c2.y);
      r1.x = 0.5f * (c1.y + c2.y); r1.y = 0.5f * (c2.x - c1.x);
      const u32x4 eu = __builtin_bit_cast(u32x4, e);
      const u32 ez = eu.z, ew = eu.w;
      const f32x2 a1 = unpk(wv[k2]), a2 = unpk(ez), b1 = unpk(wx[k2]), b2 = unpk(ew);
      f32x2 h0, h1;
      h0.x = 0.5f * (a1.x + a2.x); h0.y = 0.5f * (a1.y - a2.y);
      h1.x = 0.5f * (b1.x + b2.x); h1.y = 0.5f * (b1.y - b2.y);
      const f32x2 p0 = cmul(r0, h0), p1 = cmul(r1, h1);
      v[k2].x = p0.x - p1.y;
      v[k2].y = p0.y + p1.x;
    }
  }
  fft512_inv(v, buf, tw, lane);
  wave_sync();
#pragma unroll
  for (int j = 0; j < 8; ++j) buf[lane + 64 * j] = v[j];
  SYNC();
  {
    f32x2 y[2][4];
#pragma unroll
    for (int k = 0; k < 4; ++k) {
      const f32x4 o = *(const f32x4*)(lds + k * PS + 2 * tid);
      y[0][k] = o.xy; y[1][k] = o.zw;
      if (k) { y[0][k] = cmulc(y[0][k], t4[0][k - 1]); y[1][k] = cmulc(y[1][k], t4[1][k - 1]); }
    }
    f32x2 o[2][4];
    dft4<1>(y[0][0], y[0][1], y[0][2], y[0][3], o[0][0], o[0][1], o[0][2], o[0][3]);
    dft4<1>(y[1][0], y[1][1], y[1][2], y[1][3], o[1][0], o[1][1], o[1][2], o[1][3]);
    const rsrc_t rw2 = mkrsrc(ws);
#pragma unroll
    for (int n3 = 0; n3 < 4; ++n3) {
      f32x4 w; w.xy = o[0][n3]; w.zw = o[1][n3];
      st16(rw2, (u32)WS_ZT + (u32)(kd * SEQ * 8 + (tid + 256 * n3) * 16), __builtin_bit_cast(u32x4, w));
    }
  }
  WAIT_V(0);
  SYNC();
  u32* sync = (u32*)(ws + WS_SYNC);
  if (tid == 0) {
    if (__hip_atomic_fetch_add(sync + (kd & 7) * 16, 1u, __ATOMIC_RELAXED, __HIP_MEMORY_SCOPE_AGENT) == 31)
      __hip_atomic_fetch_add(sync + 40 * 16, 1u, __ATOMIC_RELAXED, __HIP_MEMORY_SCOPE_AGENT);
  }

  const int s0 = kd * 8;
  const int row = s0 + 2 * wave;
  const float g0 = ((const float*)(ws + WS_G))[row], g1 = ((const float*)(ws + WS_G))[row + 1];
  if (wave == 0) {
    for (int it = 0; it < (1 << 22); ++it) {
      if (__hip_atomic_load(sync + 40 * 16, __ATOMIC_RELAXED, __HIP_MEMORY_SCOPE_AGENT) >= 8) break;
      __builtin_amdgcn_s_sleep(4);
    }
  }
  SYNC();
  const rsrc_t rws = mkrsrc(ws);
  u32x4 z[4];
#pragma unroll
  for (int r = 0; r < 4; ++r) {
    const int i = tid + 256 * r;
    z[r] = __builtin_amdgcn_raw_buffer_load_b128(rws, (u32)WS_ZT + (u32)((i >> 2) * SEQ + s0 + 2 * (i & 3)) * 8, 0, 16);
  }
#pragma unroll
  for (int r = 0; r < 4; ++r) {
    const int i = tid + 256 * r;
    const int kk = i >> 2, p = i & 3;
    const f32x4 qq = __builtin_bit_cast(f32x4, z[r]);
    f32x2 c1, c2;
    if (kk) {
      c1.x = qq.x - qq.w; c1.y = qq.y + qq.z;
      c2.x = qq.x + qq.w; c2.y = qq.z - qq.y;
    } else {
      c1.x = qq.x; c1.y = qq.z;
      c2.x = qq.y; c2.y = qq.w;
    }
    lds[p * PS3 + sslot(kk)] = c1;
    lds[p * PS3 + sslot(kk ? 512 - kk : 256)] = c2;
  }
  SYNC();
  f32x2* buf3 = lds + wave * PS3;
#pragma unroll
  for (int k2 = 0; k2 < 8; ++k2) v[k2] = buf3[sslot(hi + 8 * lo + 64 * k2)];
  fft512_inv(v, buf3, tw, lane);
  wave_sync();
#pragma unroll
  for (int j = 0; j < 8; ++j) buf3[lane + 2 * (lane >> 3) + 80 * j] = v[j];
  wave_sync();
  f32x4 u[4];
#pragma unroll
  for (int j = 0; j < 4; ++j) u[j] = *(const f32x4*)(buf3 + 10 * lane + 2 * j);
  for (int it = 0; it < (1 << 22); ++it) {
    if (__hip_atomic_load(sync + (8 + (s0 >> 6)) * 16, __ATOMIC_RELAXED, __HIP_MEMORY_SCOPE_AGENT) >= 8) break;
    __builtin_amdgcn_s_sleep(2);
  }
  asm volatile("" ::: "memory");
  const u32x4 b0 = __builtin_amdgcn_raw_buffer_load_b128(rws, (u32)WS_B + (u32)(row * DM + lane * 8) * 2, 0, 16);
  const u32x4 b1 = __builtin_amdgcn_raw_buffer_load_b128(rws, (u32)WS_B + (u32)((row + 1) * DM + lane * 8) * 2, 0, 16);
  const float sc = 1.0f / (2048.0f * 512.0f);
  const float og0 = bfr(1.0f - g0), og1 = bfr(1.0f - g1);
  u32x4 o0, o1;
#pragma unroll
  for (int j = 0; j < 4; ++j) {
    const f32x2 bv0 = unpk(b0[j]), bv1 = unpk(b1[j]);
    o0[j] = pack2(bfr(g0 * bfr(u[j].x * sc)) + bfr(og0 * bv0.x), bfr(g0 * bfr(u[j].z * sc)) + bfr(og0 * bv0.y));
    o1[j] = pack2(bfr(g1 * bfr(u[j].y * sc)) + bfr(og1 * bv1.x), bfr(g1 * bfr(u[j].w * sc)) + bfr(og1 * bv1.y));
  }
  const rsrc_t ro = mkrsrc(out);
  st16(ro, (u32)(row * DM + lane * 8) * 2, o0);
  st16(ro, (u32)((row + 1) * DM + lane * 8) * 2, o1);
}

NOPK __global__ __launch_bounds__(256, 2) void k_l2(char* __restrict__ ws, u16* __restrict__ out) {
  extern __shared__ __attribute__((aligned(16))) char smem[];
  if (BIDX >= 256) g2_body(ws, smem, BIDX - 256);
  else k2_body(ws, smem, BIDX, out);
}

extern "C" void kernel_launch(void* const* d_in, const int* in_sizes, int n_in, void* d_out, int out_size,
                              void* d_ws, size_t ws_size, hipStream_t stream) {
  const u16* x = (const u16*)d_in[0];
  const u16* wr = (const u16*)d_in[1];
  const u16* wi = (const u16*)d_in[2];
  const u16* wg = (const u16*)d_in[4];
  const u16* wu = (const u16*)d_in[5];
  const u16* wd = (const u16*)d_in[6];
  const u16* gw = (const u16*)d_in[7];
  char* ws = (char*)d_ws;
  (void)hipFuncSetAttribute((const void*)k_l1, hipFuncAttributeMaxDynamicSharedMemorySize, L1_LDS);
  (void)hipFuncSetAttribute((const void*)k_l2, hipFuncAttributeMaxDynamicSharedMemorySize, L2_LDS);
  k_l1<<<256, 1024, L1_LDS, stream>>>(x, wr, wi, wg, wu, wd, gw, ws);
  k_l2<<<512, 256, L2_LDS, stream>>>(ws, (u16*)d_out);
}
```
